```python
import torch

def _softmax(x, axis=-1):
    return torch.softmax(x, dim=axis)
def _gelu(x):
    return torch.nn.functional.gelu(x, approximate="tanh")
def _silu(x):
    return torch.nn.functional.silu(x)
def _sigmoid(x):
    return torch.sigmoid(x)
def _layernorm(x, axis=-1, eps=1e-5):
    m = x.mean(dim=axis, keepdim=True)
    v = x.var(dim=axis, keepdim=True, unbiased=False)
    return (x - m) / torch.sqrt(v + eps)
def _scatter_add(tgt, idx, src, axis=0):
    return tgt.clone().index_add_(axis, idx, src)
def _segment_sum(src, seg, n):
    out = torch.zeros((n,) + src.shape[1:], dtype=src.dtype)
    return out.index_add_(0, seg, src)
def _pad(x, pw):
    flat = tuple(v for pair in reversed(pw) for v in pair)
    return torch.nn.functional.pad(x, flat)

kernel_name = 'interleave_layers__sliding_block_cached__glu_gated_reglu__decode'

def setup_inputs():

    _w = (lambda a: (lambda t: t.to(torch.bfloat16) if t.is_floating_point() else t)(torch.as_tensor(a)))
    def _A_setup():
        B, Sq, D = 4, 1, 640
        Hq, Hkv, Sk, Dh = 32, 32, 4096, 128
        assert Hq % Hkv == 0, (Hq, Hkv)
        return {
            "x": _w((lambda *s: torch.randn(*s, dtype=torch.bfloat16) * 0.05)(B, Sq, D)),
            "k_cache": _w((lambda *s: torch.randn(*s, dtype=torch.bfloat16) * 0.8)(B, Hkv, Sk, Dh)),
            "v_cache": _w((lambda *s: torch.randn(*s, dtype=torch.bfloat16) * 0.8)(B, Hkv, Sk, Dh)),
            "wq": _w((lambda *s: torch.randn(*s, dtype=torch.bfloat16) * 0.8)(D, Hq * Dh) * (D ** -0.5)),
            "wo": _w((lambda *s: torch.randn(*s, dtype=torch.bfloat16) * 0.8)(Hq * Dh, D) * ((Hq * Dh) ** -0.5)),
        }
    _A_in = _A_setup()

    def _B_setup():
        B, S, D, F = 4, 1, 640, 1728
        return {
            "x": _w((lambda *s: torch.randn(*s, dtype=torch.bfloat16) * 0.05)(B, S, D)),
            "W_gate": _w((lambda *s: torch.randn(*s, dtype=torch.bfloat16) * 0.2)(D, F) * (2.0 / D) ** 0.5),
            "W_up": _w((lambda *s: torch.randn(*s, dtype=torch.bfloat16) * 0.2)(D, F) * (2.0 / D) ** 0.5),
            "W_down": _w((lambda *s: torch.randn(*s, dtype=torch.bfloat16) * 0.2)(F, D) * (1.0 / F) ** 0.5),
        }
    _B_in = _B_setup()

    return {
        "A_x": _A_in["x"],
        "A_k_cache": _A_in["k_cache"],
        "A_v_cache": _A_in["v_cache"],
        "A_wq": _A_in["wq"],
        "A_wo": _A_in["wo"],
        "B_x": _B_in["x"],
        "B_W_gate": _B_in["W_gate"],
        "B_W_up": _B_in["W_up"],
        "B_W_down": _B_in["W_down"],
    }

def reference(A_x, A_k_cache, A_v_cache, A_wq, A_wo, B_x, B_W_gate, B_W_up, B_W_down):
    def _A_ref(x, k_cache, v_cache, wq, wo):
        B, Sq, D = x.shape
        Hq, Dh = 32, 128
        Hkv, Sk = k_cache.shape[1], k_cache.shape[2]
        W = min(4096, max(256, Sk // 4))
        rep = Hq // Hkv
        q = (lambda x, *d: x.permute(*d))((x @ wq).reshape(B, Sq, Hq, Dh), 0, 2, 1, 3)
        k = (lambda x, n, axis: torch.repeat_interleave(x, n, dim=axis))(k_cache[:, :, Sk - W:, :], rep, axis=1)
        v = (lambda x, n, axis: torch.repeat_interleave(x, n, dim=axis))(v_cache[:, :, Sk - W:, :], rep, axis=1)
        scores = torch.einsum("bhqd,bhkd->bhqk", q, k) * (Dh ** -0.5)
        p = _softmax(scores, axis=-1)
        o = torch.einsum("bhqk,bhkd->bhqd", p, v)
        return (lambda x, *d: x.permute(*d))(o, 0, 2, 1, 3).reshape(B, Sq, Hq * Dh) @ wo

    def _B_ref(x, W_gate, W_up, W_down):
        gate = torch.einsum("bsd,df->bsf", x, W_gate)
        up = torch.einsum("bsd,df->bsf", x, W_up)
        gate = gate * (lambda x, dt: x.to(dt))(gate > 0, gate.dtype)
        h = gate * up
        return torch.einsum("bsf,fd->bsd", h, W_down)

    x = A_x
    x = x + _A_ref(_layernorm(x, axis=-1), A_k_cache, A_v_cache, A_wq, A_wo)
    x = x + _B_ref(_layernorm(x, axis=-1), B_W_gate, B_W_up, B_W_down)
    x = x + _A_ref(_layernorm(x, axis=-1), A_k_cache, A_v_cache, A_wq, A_wo)
    x = x + _B_ref(_layernorm(x, axis=-1), B_W_gate, B_W_up, B_W_down)
    return x
```

```cpp
#include <hip/hip_runtime.h>
#include <hip/hip_bf16.h>
#include <stdint.h>
constexpr int B=4, D=640, H=32, SK=4096, DH=128, W=1024, F=1728, HD=H*DH;
typedef unsigned short u16;
__device__ inline float bf2f(u16 b){ return __builtin_bit_cast(float,(unsigned)b<<16); }
__device__ inline u16 f2bf(float f){ unsigned u=__builtin_bit_cast(unsigned,f); u+=0x7FFFu+((u>>16)&1u); return (u16)(u>>16);}
__device__ inline float rbf(float f){ return bf2f(f2bf(f)); }
__device__ float block_sum(float v, float* red){
  for(int o=32;o>0;o>>=1) v+=__shfl_xor(v,o);
  int w=threadIdx.x>>6; __syncthreads(); if((threadIdx.x&63)==0) red[w]=v; __syncthreads();
  float s=0; for(int i=0;i<(int)(blockDim.x>>6);++i) s+=red[i]; return s;
}
__device__ float block_max(float v, float* red){
  for(int o=32;o>0;o>>=1) v=fmaxf(v,__shfl_xor(v,o));
  int w=threadIdx.x>>6; __syncthreads(); if((threadIdx.x&63)==0) red[w]=v; __syncthreads();
  float s=-INFINITY; for(int i=0;i<(int)(blockDim.x>>6);++i) s=fmaxf(s,red[i]); return s;
}
__device__ void ln_row(const float* x, float* xn, float* red){
  float s=0; for(int i=threadIdx.x;i<D;i+=blockDim.x) s+=x[i];
  float m=block_sum(s,red)/D;
  float v=0; for(int i=threadIdx.x;i<D;i+=blockDim.x){ float d=x[i]-m; v+=d*d; }
  v=block_sum(v,red)/D;
  float r=1.0f/sqrtf(v+1e-5f);
  for(int i=threadIdx.x;i<D;i+=blockDim.x) xn[i]=rbf((x[i]-m)*r);
  __syncthreads();
}
__global__ void k_init(const u16* x, float* xf){ int i=blockIdx.x*256+threadIdx.x; if(i<B*D) xf[i]=bf2f(x[i]); }
__global__ void k_q(const float* xf, const u16* wq, float* q){
  __shared__ float xn[D]; __shared__ float red[8]; __shared__ float part[4][64];
  int b=blockIdx.y; ln_row(xf+b*D,xn,red);
  int n=blockIdx.x*64+(threadIdx.x&63), w=threadIdx.x>>6; float a=0;
  for(int k=w;k<D;k+=4) a+=xn[k]*bf2f(wq[(size_t)k*HD+n]);
  part[w][threadIdx.x&63]=a; __syncthreads();
  if(w==0) q[b*HD+n]=rbf(part[0][n&63]+part[1][n&63]+part[2][n&63]+part[3][n&63]);
}
__global__ void k_attn(const float* q, const u16* K, const u16* V, float* o){
  __shared__ float s[W]; __shared__ float red[8]; __shared__ float qs[DH]; __shared__ float part[2][DH];
  int h=blockIdx.x,b=blockIdx.y; const u16* Kb=K+((size_t)(b*H+h)*SK+(SK-W))*DH; const u16* Vb=V+((size_t)(b*H+h)*SK+(SK-W))*DH;
  if(threadIdx.x<DH) qs[threadIdx.x]=q[b*HD+h*DH+threadIdx.x]; __syncthreads();
  float mx=-INFINITY;
  for(int j=threadIdx.x;j<W;j+=256){ float a=0; for(int d=0;d<DH;++d) a+=qs[d]*bf2f(Kb[(size_t)j*DH+d]); a*=0.08838834764831845f; s[j]=a; mx=fmaxf(mx,a);}
  mx=block_max(mx,red); float l=0;
  for(int j=threadIdx.x;j<W;j+=256){ float e=__expf(s[j]-mx); s[j]=e; l+=e; }
  l=block_sum(l,red);
  int d=threadIdx.x&127, g=threadIdx.x>>7; float a=0;
  for(int j=g;j<W;j+=2) a+=s[j]*bf2f(Vb[(size_t)j*DH+d]);
  part[g][d]=a; __syncthreads();
  if(g==0) o[b*HD+h*DH+d]=rbf((part[0][d]+part[1][d])/l);
}
template<int KK,int N,bool FINAL> __global__ void k_res(const float* in, const u16* Wt, float* xf, u16* out){
  __shared__ float part[16][17];
  int b=blockIdx.y, n=blockIdx.x*16+(threadIdx.x&15), g=threadIdx.x>>4; float a=0;
  for(int k=g;k<KK;k+=16) a+=in[b*KK+k]*bf2f(Wt[(size_t)k*N+n]);
  part[g][threadIdx.x&15]=a; __syncthreads();
  if(g==0){ float s=0; for(int i=0;i<16;++i) s+=part[i][n&15]; float r=xf[b*N+n]+s; xf[b*N+n]=r; if(FINAL) out[b*N+n]=f2bf(r);}
}
__global__ void k_gu(const float* xf, const u16* wg, const u16* wu, float* hh){
  __shared__ float xn[D]; __shared__ float red[8]; __shared__ float pg[4][64], pu[4][64];
  int b=blockIdx.y; ln_row(xf+b*D,xn,red);
  int n=blockIdx.x*64+(threadIdx.x&63), w=threadIdx.x>>6; float g=0,u=0;
  for(int k=w;k<D;k+=4){ g+=xn[k]*bf2f(wg[(size_t)k*F+n]); u+=xn[k]*bf2f(wu[(size_t)k*F+n]); }
  pg[w][n&63]=g; pu[w][n&63]=u; __syncthreads();
  if(w==0){ g=rbf(pg[0][n&63]+pg[1][n&63]+pg[2][n&63]+pg[3][n&63]); u=rbf(pu[0][n&63]+pu[1][n&63]+pu[2][n&63]+pu[3][n&63]); hh[b*F+n]=rbf((g>0?g:0.f)*u);}
}
extern "C" void kernel_launch(void* const* d_in, const int* in_sizes, int n_in, void* d_out, int out_size, void* d_ws, size_t ws_size, hipStream_t stream){
  const u16 *x=(const u16*)d_in[0],*K=(const u16*)d_in[1],*V=(const u16*)d_in[2],*wq=(const u16*)d_in[3],*wo=(const u16*)d_in[4];
  const u16 *wg=(const u16*)d_in[6],*wu=(const u16*)d_in[7],*wd=(const u16*)d_in[8];
  float* xf=(float*)d_ws; float* q=xf+B*D; float* o=q+B*HD; float* hh=o+B*HD;
  k_init<<<(B*D+255)/256,256,0,stream>>>(x,xf);
  for(int l=0;l<2;++l){
    k_q<<<dim3(HD/64,B),256,0,stream>>>(xf,wq,q);
    k_attn<<<dim3(H,B),256,0,stream>>>(q,K,V,o);
    k_res<HD,D,false><<<dim3(D/16,B),256,0,stream>>>(o,wo,xf,(u16*)d_out);
    k_gu<<<dim3(F/64,B),256,0,stream>>>(xf,wg,wu,hh);
    if(l==0) k_res<F,D,false><<<dim3(D/16,B),256,0,stream>>>(hh,wd,xf,(u16*)d_out);
    else k_res<F,D,true><<<dim3(D/16,B),256,0,stream>>>(hh,wd,xf,(u16*)d_out);
  }
}
```

```cpp
#include <hip/hip_runtime.h>
#include <stdint.h>

#define MK_STAMPS 0

namespace {
typedef unsigned short u16;
typedef unsigned v4u __attribute__((ext_vector_type(4)));
typedef unsigned v2u __attribute__((ext_vector_type(2)));
typedef float f4 __attribute__((ext_vector_type(4)));
typedef float f2 __attribute__((ext_vector_type(2)));
typedef __bf16 bf8 __attribute__((ext_vector_type(8)));
typedef __bf16 bf2 __attribute__((ext_vector_type(2)));
typedef short s4 __attribute__((ext_vector_type(4)));
typedef __amdgpu_buffer_rsrc_t rsrc_t;
#define LAS __attribute__((address_space(3)))

constexpr int NB = 4, D = 640, NH = 32, DH = 128, SK = 4096, WIN = 1024, FF = 1728, HD = 4096;
constexpr unsigned SENT = 0xFFFFFFFFu;
constexpr int OOB = 0x40000000;
constexpr int SZ_Q = 32 * 4 * 256, SZ_O = 32 * 4 * 256, SZ_X1 = 4 * 40 * 128, SZ_H = 108 * 128, SZ_X2 = 40 * 128;
constexpr int OFF_Q = 0, OFF_O = OFF_Q + 2 * SZ_Q, OFF_X1 = OFF_O + 2 * SZ_O, OFF_H = OFF_X1 + 2 * SZ_X1,
              OFF_X2 = OFF_H + 2 * SZ_H, WS_USED = OFF_X2 + SZ_X2;
constexpr int OFF_STAMP = 1 << 20;
constexpr int SPIN_CAP = 1 << 17;
constexpr int L_V = 0;
constexpr int L_P = 131072;
constexpr int L_OW = L_P + 2048;
constexpr int L_ML = L_OW + 4096;
constexpr int L_END = L_ML + 64;
constexpr int L_RED = 0;
constexpr int RED_W = 5 * 36 * 4;

constexpr int AUX_NT = 2, AUX_SC1 = 16;

__device__ __forceinline__ f4 mfma(v4u a, v4u b, f4 c) {
  return __builtin_amdgcn_mfma_f32_16x16x32_bf16(__builtin_bit_cast(bf8, a), __builtin_bit_cast(bf8, b), c, 0, 0, 0);
}
__device__ __forceinline__ unsigned hi16pack(float hi, float lo) {
  return __builtin_amdgcn_perm(__builtin_bit_cast(unsigned, hi), __builtin_bit_cast(unsigned, lo), 0x07060302u);
}
__device__ __forceinline__ unsigned pack_bf16(float lo, float hi) {
  bf2 r = {(__bf16)lo, (__bf16)hi};
  unsigned u = __builtin_bit_cast(unsigned, r);
  return u == SENT ? 0x7FFF7FFFu : u;
}
__device__ __forceinline__ float bflo(unsigned u) { return __builtin_bit_cast(float, u << 16); }
__device__ __forceinline__ float bfhi(unsigned u) { return __builtin_bit_cast(float, u & 0xFFFF0000u); }
__device__ __forceinline__ float dot2(unsigned a, unsigned b, float c) {
  return __builtin_amdgcn_fdot2_f32_bf16(__builtin_bit_cast(bf2, a), __builtin_bit_cast(bf2, b), c, false);
}
template <int CTRL> __device__ __forceinline__ float dpp(float x) {
  return __builtin_bit_cast(float, __builtin_amdgcn_mov_dpp(__builtin_bit_cast(int, x), CTRL, 0xf, 0xf, true));
}
constexpr int XOR1 = 0xB1, XOR2 = 0x4E, ROR4 = 0x124, ROR8 = 0x128, SHL4 = 0x104;
__device__ __forceinline__ float xrow_sum(float x) {
  auto s = __builtin_amdgcn_permlane16_swap(__float_as_uint(x), __float_as_uint(x), false, false);
  x = __uint_as_float(s[0]) + __uint_as_float(s[1]);
  auto t = __builtin_amdgcn_permlane32_swap(__float_as_uint(x), __float_as_uint(x), false, false);
  return __uint_as_float(t[0]) + __uint_as_float(t[1]);
}
__device__ __forceinline__ float xrow_max(float x) {
  auto s = __builtin_amdgcn_permlane16_swap(__float_as_uint(x), __float_as_uint(x), false, false);
  x = fmaxf(__uint_as_float(s[0]), __uint_as_float(s[1]));
  auto t = __builtin_amdgcn_permlane32_swap(__float_as_uint(x), __float_as_uint(x), false, false);
  return fmaxf(__uint_as_float(t[0]), __uint_as_float(t[1]));
}
__device__ __forceinline__ float x32_sum(float x) {
  auto t = __builtin_amdgcn_permlane32_swap(__float_as_uint(x), __float_as_uint(x), false, false);
  return __uint_as_float(t[0]) + __uint_as_float(t[1]);
}
__device__ __forceinline__ rsrc_t mkrsrc(const void* p, unsigned bytes) {
  return __builtin_amdgcn_make_buffer_rsrc((void*)p, 0, bytes, 0x00020000);
}
template <int AUX> __device__ __forceinline__ v4u ld128(rsrc_t rs, int voff, int soff) {
  return __builtin_amdgcn_raw_buffer_load_b128(rs, voff, soff, AUX);
}
__device__ __forceinline__ unsigned umax3(unsigned a, unsigned b, unsigned c) { return max(max(a, b), c); }
__device__ __forceinline__ unsigned vmax4(v4u x, unsigned m) { return umax3(x.x, x.y, umax3(x.z, x.w, m)); }

template <int N> __device__ __forceinline__ void gather(v4u (&x)[N], rsrc_t rs, const int (&off)[N], bool& dead) {
  for (int spin = 0;; ++spin) {
#pragma unroll
    for (int i = 0; i < N; ++i) x[i] = ld128<AUX_SC1>(rs, off[i], 0);
    unsigned m = 0u;
#pragma unroll
    for (int i = 0; i < N; ++i) m = vmax4(x[i], m);
    if (!__any(m == SENT)) break;
    if (dead || spin > SPIN_CAP) { dead = true; break; }
    __builtin_amdgcn_s_sleep(2);
  }
}
__device__ __forceinline__ v4u xpose16(v4u raw, v4u sel1, v4u sel2) {
  const f4 z = {0.f, 0.f, 0.f, 0.f};
  const f4 d1 = mfma(raw, sel1, z), d2 = mfma(raw, sel2, z);
  return v4u{hi16pack(d1[1], d1[0]), hi16pack(d1[3], d1[2]), hi16pack(d2[1], d2[0]), hi16pack(d2[3], d2[2])};
}
__device__ __forceinline__ void xpose32(v4u& a, v4u& b, v4u sel1, v4u sel2) {
  const f4 z = {0.f, 0.f, 0.f, 0.f};
  const f4 a1 = mfma(a, sel1, z), b1 = mfma(b, sel1, z), a2 = mfma(a, sel2, z), b2 = mfma(b, sel2, z);
  a = v4u{hi16pack(a1[1], a1[0]), hi16pack(a1[3], a1[2]), hi16pack(b1[1], b1[0]), hi16pack(b1[3], b1[2])};
  b = v4u{hi16pack(a2[1], a2[0]), hi16pack(a2[3], a2[2]), hi16pack(b2[1], b2[0]), hi16pack(b2[3], b2[2])};
}

#define STAMP(i) do { } while (0)

#define ASM_LD(dst, voff, rs, soff, MODS) \
  asm volatile("buffer_load_dwordx4 %0, %1, %2, %3 offen " MODS : "=v"(dst) : "v"(voff), "s"(rs), "s"(soff) : "memory")
#define ASM_DMA(voff, rs, soff, ldsa) \
  asm volatile("s_mov_b32 m0, %3\n\ts_nop 0\n\tbuffer_load_dwordx4 %0, %1, %2 offen nt lds" ::"v"(voff), "s"(rs), "s"(soff), "s"(ldsa) : "memory")
#define WAIT_VM(n) asm volatile("s_waitcnt vmcnt(" #n ")" ::: "memory")
#define PIN8(a, o) asm volatile("" : "+v"(a[o]), "+v"(a[o + 1]), "+v"(a[o + 2]), "+v"(a[o + 3]), "+v"(a[o + 4]), "+v"(a[o + 5]), "+v"(a[o + 6]), "+v"(a[o + 7]))

__device__ __forceinline__ v2u tr_read(LAS unsigned char* p) {
  s4 t = __builtin_amdgcn_ds_read_tr16_b64_v4i16((LAS s4*)p);
  return __builtin_bit_cast(v2u, t);
}

template <int PASS>
__device__ __forceinline__ void attn_math(LAS unsigned char* L, const v4u (&kf)[32], const v4u (&vf)[16], const v4u (&qf)[4], rsrc_t rws,
                                          int b, int h, int w, int l, unsigned long long (&st)[16]) {
  const int g = l >> 4;
  const f4 z = {0.f, 0.f, 0.f, 0.f};
  const v4u ones = {0x3F803F80u, 0x3F803F80u, 0x3F803F80u, 0x3F803F80u};
  constexpr float CS = 0.08838834764831845f * 1.4426950408889634f;
  float s0 = 0.f, s1 = 0.f;
#pragma unroll
  for (int t = 0; t < 8; ++t) {
    f4 d = z;
#pragma unroll
    for (int s = 0; s < 4; ++s) d = mfma(qf[s], kf[t * 4 + s], d);
    if (t < 4) s0 = (g == t) ? d[0] : s0; else s1 = (g == t - 4) ? d[0] : s1;
  }
  float m = fmaxf(s0, s1);
  m = fmaxf(m, dpp<XOR1>(m)); m = fmaxf(m, dpp<XOR2>(m)); m = fmaxf(m, dpp<ROR4>(m)); m = fmaxf(m, dpp<ROR8>(m));
  m = xrow_max(m);
  const float p0 = __builtin_amdgcn_exp2f((s0 - m) * CS), p1 = __builtin_amdgcn_exp2f((s1 - m) * CS);
  LAS __bf16* pw = (LAS __bf16*)(L + L_P + w * 256);
  pw[l] = (__bf16)p0; pw[64 + l] = (__bf16)p1;
  v4u pf[4];
#pragma unroll
  for (int s = 0; s < 4; ++s) pf[s] = *(LAS v4u*)(L + L_P + w * 256 + s * 64 + g * 16);
  float oa0 = 0.f, oa1 = 0.f;
#pragma unroll
  for (int n = 0; n < 8; ++n) {
    f4 d = mfma(pf[0], vf[n], z);
    d = mfma(pf[1], vf[8 + n], d);
    if (n < 4) oa0 = (g == n) ? d[0] : oa0; else oa1 = (g == n - 4) ? d[0] : oa1;
  }
  f4 dl = z;
#pragma unroll
  for (int s = 0; s < 4; ++s) dl = mfma(pf[s], ones, dl);
  if (PASS == 0) WAIT_VM(0);
  STAMP(PASS == 0 ? 2 : 6);
  const int q4 = (l >> 2) & 3, p4 = l & 3;
  const int trb = L_V + w * 16384 + (8 * g + q4) * 256 + ((q4 | ((g & 1) << 2)) << 5) + p4 * 8;
  float ob0 = 0.f, ob1 = 0.f;
#pragma unroll
  for (int n = 0; n < 8; ++n) {
    LAS unsigned char* a = L + (trb ^ (n << 5));
    const v2u t0 = tr_read(a), t1 = tr_read(a + 1024), t2 = tr_read(a + 8192), t3 = tr_read(a + 8192 + 1024);
    f4 d = mfma(pf[2], v4u{t0.x, t0.y, t1.x, t1.y}, z);
    d = mfma(pf[3], v4u{t2.x, t2.y, t3.x, t3.y}, d);
    if (n < 4) ob0 = (g == n) ? d[0] : ob0; else ob1 = (g == n - 4) ? d[0] : ob1;
  }
  LAS float* ow = (LAS float*)(L + L_OW);
  ow[w * 128 + l] = oa0 + ob0; ow[w * 128 + 64 + l] = oa1 + ob1;
  if (l == 0) *(LAS f2*)(L + L_ML + w * 8) = f2{m, dl[0]};
  __syncthreads();
  if (w == 0) {
    f2 ml[8], ov[8];
#pragma unroll
    for (int i = 0; i < 8; ++i) { ml[i] = *(LAS f2*)(L + L_ML + i * 8); ov[i] = *(LAS f2*)(L + L_OW + i * 512 + l * 8); }
    float M = ml[0].x;
#pragma unroll
    for (int i = 1; i < 8; ++i) M = fmaxf(M, ml[i].x);
    float Ls = 0.f, o0 = 0.f, o1 = 0.f;
#pragma unroll
    for (int i = 0; i < 8; ++i) {
      const float f = __builtin_amdgcn_exp2f((ml[i].x - M) * CS);
      Ls = fmaf(f, ml[i].y, Ls); o0 = fmaf(f, ov[i].x, o0); o1 = fmaf(f, ov[i].y, o1);
    }
    const float inv = 1.0f / Ls;
    __builtin_amdgcn_raw_buffer_store_b32(pack_bf16(o0 * inv, o1 * inv), rws, OFF_O + PASS * SZ_O + (h * 4 + b) * 256 + l * 4, 0, AUX_SC1);
  }
  STAMP(PASS == 0 ? 3 : 7);
}

__device__ __forceinline__ void attn_role(LAS unsigned char* L, const u16* K, const u16* V, rsrc_t rws, int xc, int y, int w, int l,
                                          unsigned long long (&st)[16]) {
  const int h = 4 * xc + (y >> 2), b = y & 3, g = l >> 4, r = l & 15;
  const size_t kv = ((size_t)(b * NH + h) * SK + (SK - WIN) + 128 * w) * DH;
  const rsrc_t rK = mkrsrc(K + kv, 32768), rV = mkrsrc(V + kv, 32768);
  v4u kf[32], vf[16], qf[4];
  const int kvo = r * 256 + g * 16;
  const int vs0 = g * 256 + (((r >> 1) ^ g) << 5) + (l & 1) * 16, vs1 = g * 256 + (((r >> 1) ^ (g | 4)) << 5) + (l & 1) * 16;
  const int ldsV = L_V + w * 16384;
  const int qo = OFF_Q + (h * 16 + b) * 64 + g * 16;
#pragma unroll
  for (int i = 0; i < 16; ++i) ASM_DMA((i & 2) ? vs1 : vs0, rV, i * 1024, ldsV + i * 1024);
#pragma unroll
  for (int i = 0; i < 32; ++i) ASM_LD(kf[i], kvo, rK, (i >> 2) * 4096 + (i & 3) * 64, "nt");
#pragma unroll
  for (int s = 0; s < 4; ++s) ASM_LD(qf[s], qo, rws, s * 256, "sc1");
  WAIT_VM(36);
  {
    const int q4 = (l >> 2) & 3, p4 = l & 3;
    const int trb = ldsV + (8 * g + q4) * 256 + ((q4 | ((g & 1) << 2)) << 5) + p4 * 8;
#pragma unroll
    for (int n = 0; n < 8; ++n) {
      LAS unsigned char* a = L + (trb ^ (n << 5));
      const v2u t0 = tr_read(a), t1 = tr_read(a + 1024), t2 = tr_read(a + 8192), t3 = tr_read(a + 8192 + 1024);
      vf[n] = v4u{t0.x, t0.y, t1.x, t1.y};
      vf[8 + n] = v4u{t2.x, t2.y, t3.x, t3.y};
    }
    asm volatile("s_waitcnt lgkmcnt(0)" ::: "memory");
    PIN8(vf, 0); PIN8(vf, 8);
  }
#pragma unroll
  for (int i = 0; i < 16; ++i) ASM_DMA((i & 2) ? vs1 : vs0, rV, 16384 + i * 1024, ldsV + i * 1024);
  WAIT_VM(16);
  PIN8(kf, 0); PIN8(kf, 8); PIN8(kf, 16); PIN8(kf, 24);
  asm volatile("" : "+v"(qf[0]), "+v"(qf[1]), "+v"(qf[2]), "+v"(qf[3]));
  STAMP(1);
  bool dead = false;
  if (__any(vmax4(qf[0], vmax4(qf[1], vmax4(qf[2], vmax4(qf[3], 0u)))) == SENT)) {
    const int off[4] = {qo, qo + 256, qo + 512, qo + 768};
    gather<4>(qf, rws, off, dead);
  }
  attn_math<0>(L, kf, vf, qf, rws, b, h, w, l, st);
  {
    const int off[4] = {qo + SZ_Q, qo + SZ_Q + 256, qo + SZ_Q + 512, qo + SZ_Q + 768};
    gather<4>(qf, rws, off, dead);
  }
  STAMP(5);
  attn_math<1>(L, kf, vf, qf, rws, b, h, w, l, st);
}

__device__ __forceinline__ f4 red_sum(LAS unsigned char* L, int row, int l) {
  const int hf = l >> 5, cg = l & 7;
  f4 s = *(LAS f4*)(L + L_RED + (hf * 4) * RED_W + row * 144 + cg * 16);
#pragma unroll
  for (int i = 1; i < 4; ++i) s += *(LAS f4*)(L + L_RED + (hf * 4 + i) * RED_W + row * 144 + cg * 16);
  return f4{x32_sum(s[0]), x32_sum(s[1]), x32_sum(s[2]), x32_sum(s[3])};
}
__device__ __forceinline__ f2 red_stat(LAS unsigned char* L, int row, int l) {
  const int hf = l >> 5;
  f2 s = *(LAS f2*)(L + L_RED + (hf * 4) * RED_W + row * 144 + 128);
#pragma unroll
  for (int i = 1; i < 4; ++i) s += *(LAS f2*)(L + L_RED + (hf * 4 + i) * RED_W + row * 144 + 128);
  return f2{x32_sum(s[0]), x32_sum(s[1])};
}

struct WArgs { const u16 *x, *wq, *wo, *wg, *wu, *wd; u16* out; };

template <int PASS>
__device__ __forceinline__ void q_phase(LAS unsigned char* L, const v4u (&xf)[3], const v4u (&wqf)[6], f4 csq, rsrc_t rws, int tq, int w, int l) {
  const int g = l >> 4, c = l & 15;
  const f4 z = {0.f, 0.f, 0.f, 0.f};
  const v4u ones = {0x3F803F80u, 0x3F803F80u, 0x3F803F80u, 0x3F803F80u};
  f4 d0 = z, d1 = z, ds = z;
  float ss = 0.f;
#pragma unroll
  for (int i = 0; i < 3; ++i) {
    d0 = mfma(wqf[2 * i], xf[i], d0); d1 = mfma(wqf[2 * i + 1], xf[i], d1); ds = mfma(ones, xf[i], ds);
    ss = dot2(xf[i].x, xf[i].x, ss); ss = dot2(xf[i].y, xf[i].y, ss); ss = dot2(xf[i].z, xf[i].z, ss); ss = dot2(xf[i].w, xf[i].w, ss);
  }
  ss = xrow_sum(ss);
  LAS unsigned char* R = L + L_RED + w * RED_W + c * 144;
  if (c < 4) {
    *(LAS f4*)(R + g * 16) = d0; *(LAS f4*)(R + 64 + g * 16) = d1;
    if (g == 0) *(LAS f2*)(R + 128) = f2{ds[0], ss};
  }
  __syncthreads();
  if (w == 0) {
    const int b = (l >> 3) & 3, cg = l & 7;
    const f4 p = red_sum(L, b, l);
    const f2 s = red_stat(L, b, l);
    const float mu = s[0] * (1.0f / D), var = fmaxf(s[1] * (1.0f / D) - mu * mu, 0.f), rs = __builtin_amdgcn_rsqf(var + 1e-5f);
    if (l < 32) {
      const v2u o = {pack_bf16((p[0] - mu * csq[0]) * rs, (p[1] - mu * csq[1]) * rs), pack_bf16((p[2] - mu * csq[2]) * rs, (p[3] - mu * csq[3]) * rs)};
      __builtin_amdgcn_raw_buffer_store_b64(o, rws, OFF_Q + PASS * SZ_Q + tq * 256 + b * 64 + cg * 8, 0, AUX_SC1);
    }
  }
  __syncthreads();
}

__device__ __forceinline__ void weight_role(LAS unsigned char* L, const WArgs& a, rsrc_t rws, int xc, int u, int w, int l,
                                            unsigned long long (&st)[16]) {
  const int g = l >> 4, r = l & 15, c = r;
  const f4 z = {0.f, 0.f, 0.f, 0.f};
  const v4u ones = {0x3F803F80u, 0x3F803F80u, 0x3F803F80u, 0x3F803F80u};
  v4u sel1, sel2;
  {
    const int j = r - 8 * (g & 1);
    const unsigned one = (j & 1) ? 0x3F800000u : 0x00003F80u;
    const bool in = (unsigned)j < 8u;
    v4u s = {(in && (j >> 1) == 0) ? one : 0u, (in && (j >> 1) == 1) ? one : 0u, (in && (j >> 1) == 2) ? one : 0u, (in && (j >> 1) == 3) ? one : 0u};
    const v4u zz = {0u, 0u, 0u, 0u};
    sel1 = g < 2 ? s : zz; sel2 = g < 2 ? zz : s;
  }
  const int tq = xc * 16 + u;
  const bool has_wo = u < 10, has_gu = xc < 3 || u < 12, has_dn = u >= 12 || (xc >= 4 && u >= 10);
  const int wo_id = xc * 10 + u, kq = wo_id / 20, nt32 = wo_id % 20;
  const int t16 = xc < 3 ? xc * 16 + u : 48 + (xc - 3) * 12 + u;
  const int td = u >= 12 ? xc * 4 + (u - 12) : 32 + (xc - 4) * 2 + (u - 10);
  const int rho16 = 8 * (r >> 2) + (r & 3);
  const int rho32 = rho16 + 4 * (g >> 1);

  v4u wqf[6], xf[3];
  {
    const rsrc_t rx = mkrsrc(a.x, NB * D * 2), rq = mkrsrc(a.wq, D * HD * 2);
#pragma unroll
    for (int i = 0; i < 3; ++i) {
      const int s = w + 8 * i;
      xf[i] = ld128<0>(rx, (c < 4 && s < 20) ? c * 1280 + s * 64 + g * 16 : OOB, 0);
    }
#pragma unroll
    for (int i = 0; i < 3; ++i) {
      const int s = w + 8 * i;
      const int vo = s < 20 ? (32 * s + rho16) * 8192 + tq * 64 + g * 16 : OOB;
      wqf[2 * i] = ld128<AUX_NT>(rq, vo, 0); wqf[2 * i + 1] = ld128<AUX_NT>(rq, vo, 4 * 8192);
    }
    __builtin_amdgcn_sched_barrier(0);
  }
#pragma unroll
  for (int i = 0; i < 3; ++i) xpose32(wqf[2 * i], wqf[2 * i + 1], sel1, sel2);
  f4 csq;
  {
    f4 c0 = z, c1 = z;
#pragma unroll
    for (int i = 0; i < 3; ++i) { c0 = mfma(wqf[2 * i], ones, c0); c1 = mfma(wqf[2 * i + 1], ones, c1); }
    LAS unsigned char* R = L + L_RED + w * RED_W + 4 * 144;
    if (c == 0) { *(LAS f4*)(R + g * 16) = c0; *(LAS f4*)(R + 64 + g * 16) = c1; }
    __syncthreads();
    csq = red_sum(L, 4, l);
    __syncthreads();
  }
  q_phase<0>(L, xf, wqf, csq, rws, tq, w, l);
  STAMP(1);

  v4u wof[8], guf[6], wdf[7];
  {
    const rsrc_t ro = mkrsrc(a.wo, D * HD * 2), rg = mkrsrc(a.wg, D * FF * 2), ru = mkrsrc(a.wu, D * FF * 2), rd = mkrsrc(a.wd, D * FF * 2);
    const int vo = has_wo ? ((8 * kq + w) * 128 + rho16) * 1280 + nt32 * 64 + g * 16 : OOB;
#pragma unroll
    for (int s = 0; s < 4; ++s) { wof[2 * s] = ld128<AUX_NT>(ro, vo, s * 32 * 1280); wof[2 * s + 1] = ld128<AUX_NT>(ro, vo, (s * 32 + 4) * 1280); }
#pragma unroll
    for (int i = 0; i < 3; ++i) {
      const int s = w + 8 * i;
      const int vg = (has_gu && s < 20) ? (32 * s + rho32) * 3456 + t16 * 32 + (g & 1) * 16 : OOB;
      guf[2 * i] = ld128<AUX_NT>(rg, vg, 0); guf[2 * i + 1] = ld128<AUX_NT>(ru, vg, 0);
    }
#pragma unroll
    for (int i = 0; i < 7; ++i) {
      const int s = w + 8 * i;
      wdf[i] = ld128<AUX_NT>(rd, (has_dn && s < 54) ? (32 * s + rho32) * 1280 + td * 32 + (g & 1) * 16 : OOB, 0);
    }
    __builtin_amdgcn_sched_barrier(0);
  }
#pragma unroll
  for (int s = 0; s < 4; ++s) xpose32(wof[2 * s], wof[2 * s + 1], sel1, sel2);
#pragma unroll
  for (int i = 0; i < 6; ++i) guf[i] = xpose16(guf[i], sel1, sel2);
#pragma unroll
  for (int i = 0; i < 7; ++i) wdf[i] = xpose16(wdf[i], sel1, sel2);
  f4 csg;
  {
    f4 c0 = z, c1 = z;
#pragma unroll
    for (int i = 0; i < 3; ++i) { c0 = mfma(guf[2 * i], ones, c0); c1 = mfma(guf[2 * i + 1], ones, c1); }
    LAS unsigned char* R = L + L_RED + w * RED_W + 4 * 144;
    if (c == 0) { *(LAS f4*)(R + g * 16) = c0; *(LAS f4*)(R + 64 + g * 16) = c1; }
    __syncthreads();
    csg = red_sum(L, 4, l);
    __syncthreads();
  }
  STAMP(2);
  bool dead = false;
  const int fb = (l >> 3) & 3, fcg = l & 7;
  LAS unsigned char* R = L + L_RED + w * RED_W + c * 144;

#pragma unroll
  for (int pass = 0; pass < 2; ++pass) {
    if (has_wo) {
      v4u of[4];
      const int o0 = c < 4 ? OFF_O + pass * SZ_O + ((8 * kq + w) * 4 + c) * 256 + g * 16 : OOB;
      const int off[4] = {o0, o0 + 64, o0 + 128, o0 + 192};
      v2u res = {0u, 0u};
      if (w == 0 && kq == 0 && l < 32) {
        if (pass == 0) res = *(const v2u*)(a.x + fb * D + nt32 * 32 + fcg * 4);
        else res = __builtin_amdgcn_raw_buffer_load_b64(rws, OFF_X2 + ((2 * nt32 + (fcg >> 2)) * 4 + fb) * 32 + (fcg & 3) * 8, 0, AUX_SC1);
      }
      gather<4>(of, rws, off, dead);
      STAMP(pass == 0 ? 3 : 9);
      f4 d0 = z, d1 = z;
#pragma unroll
      for (int s = 0; s < 4; ++s) { d0 = mfma(wof[2 * s], of[s], d0); d1 = mfma(wof[2 * s + 1], of[s], d1); }
      if (c < 4) { *(LAS f4*)(R + g * 16) = d0; *(LAS f4*)(R + 64 + g * 16) = d1; }
      __syncthreads();
      if (w == 0) {
        f4 p = red_sum(L, fb, l);
        p[0] += bflo(res.x); p[1] += bfhi(res.x); p[2] += bflo(res.y); p[3] += bfhi(res.y);
        if (l < 32) {
          const v2u o = {pack_bf16(p[0], p[1]), pack_bf16(p[2], p[3])};
          __builtin_amdgcn_raw_buffer_store_b64(o, rws, OFF_X1 + pass * SZ_X1 + ((kq * 40 + 2 * nt32 + (fcg >> 2)) * 4 + fb) * 32 + (fcg & 3) * 8, 0, AUX_SC1);
        }
      }
      __syncthreads();
    }
    int xoff[3];
#pragma unroll
    for (int i = 0; i < 3; ++i) {
      const int s = w + 8 * i;
      xoff[i] = s < 20 ? OFF_X1 + pass * SZ_X1 + (((c >> 2) * 40 + 2 * s + (g >> 1)) * 4 + (c & 3)) * 32 + (g & 1) * 16 : OOB;
    }
    if (has_gu) {
      v4u x1[3];
      gather<3>(x1, rws, xoff, dead);
      STAMP(pass == 0 ? 4 : 10);
      f4 dg = z, du = z, ds = z;
#pragma unroll
      for (int i = 0; i < 3; ++i) { dg = mfma(guf[2 * i], x1[i], dg); du = mfma(guf[2 * i + 1], x1[i], du); ds = mfma(ones, x1[i], ds); }
#pragma unroll
      for (int i = 0; i < 4; ++i) {
        dg[i] += dpp<ROR4>(dg[i]); dg[i] += dpp<ROR8>(dg[i]);
        du[i] += dpp<ROR4>(du[i]); du[i] += dpp<ROR8>(du[i]);
      }
      float sx = ds[0]; sx += dpp<ROR4>(sx); sx += dpp<ROR8>(sx);
      if (c < 4) {
        *(LAS f4*)(R + g * 16) = dg; *(LAS f4*)(R + 64 + g * 16) = du;
        if (g == 0) *(LAS f2*)(R + 128) = f2{sx, 0.f};
      }
      __syncthreads();
      if (w == 0) {
        const f4 p = red_sum(L, fb, l);
        const f2 s = red_stat(L, fb, l);
        const float mu = s[0] * (1.0f / D);
        f4 v, uu;
#pragma unroll
        for (int i = 0; i < 4; ++i) { v[i] = p[i] - mu * csg[i]; uu[i] = dpp<SHL4>(v[i]); }
        if (l < 32 && fcg < 4) {
          const v2u o = {pack_bf16(fmaxf(v[0], 0.f) * uu[0], fmaxf(v[1], 0.f) * uu[1]), pack_bf16(fmaxf(v[2], 0.f) * uu[2], fmaxf(v[3], 0.f) * uu[3])};
          __builtin_amdgcn_raw_buffer_store_b64(o, rws, OFF_H + pass * SZ_H + (t16 * 4 + fb) * 32 + fcg * 8, 0, AUX_SC1);
        }
      }
      __syncthreads();
    }
    if (has_dn) {
      v4u x1[3];
      gather<3>(x1, rws, xoff, dead);
      f4 gram = z, ds = z;
#pragma unroll
      for (int i = 0; i < 3; ++i) { gram = mfma(x1[i], x1[i], gram); ds = mfma(ones, x1[i], ds); }
      float sq = (c & 3) == 0 ? gram[0] : (c & 3) == 1 ? gram[1] : (c & 3) == 2 ? gram[2] : gram[3];
      sq += dpp<ROR4>(sq); sq += dpp<ROR8>(sq); sq = xrow_sum(sq);
      float sx = ds[0]; sx += dpp<ROR4>(sx); sx += dpp<ROR8>(sx);
      if (c < 4 && g == 0) *(LAS f2*)(R + 128) = f2{sx, sq};
      v2u rp[4];
      __syncthreads();
      if (w == 0) {
#pragma unroll
        for (int k = 0; k < 4; ++k)
          rp[k] = __builtin_amdgcn_raw_buffer_load_b64(rws, OFF_X1 + pass * SZ_X1 + ((k * 40 + td) * 4 + fb) * 32 + (fcg & 3) * 8, 0, AUX_SC1);
      }
      float rs2 = 0.f; f4 res = z;
      if (w == 0) {
        const f2 s = red_stat(L, fb, l);
        const float mu = s[0] * (1.0f / D), var = fmaxf(s[1] * (1.0f / D) - mu * mu, 0.f);
        rs2 = 1.0f / (var + 1e-5f);
#pragma unroll
        for (int k = 0; k < 4; ++k) { res[0] += bflo(rp[k].x); res[1] += bfhi(rp[k].x); res[2] += bflo(rp[k].y); res[3] += bfhi(rp[k].y); }
      }
      __syncthreads();
      v4u hf[7];
      int hoff[7];
#pragma unroll
      for (int i = 0; i < 7; ++i) {
        const int s = w + 8 * i;
        hoff[i] = (c < 4 && s < 54) ? OFF_H + pass * SZ_H + ((2 * s + (g >> 1)) * 4 + c) * 32 + (g & 1) * 16 : OOB;
      }
      gather<7>(hf, rws, hoff, dead);
      STAMP(pass == 0 ? 5 : 11);
      f4 d = z;
#pragma unroll
      for (int i = 0; i < 7; ++i) d = mfma(wdf[i], hf[i], d);
      if (c < 4) { *(LAS f4*)(R + g * 16) = d; *(LAS f4*)(R + 64 + g * 16) = z; }
      __syncthreads();
      if (w == 0) {
        const f4 p = red_sum(L, fb, l);
        if (l < 32 && fcg < 4) {
          f4 o;
#pragma unroll
          for (int i = 0; i < 4; ++i) o[i] = res[i] + rs2 * p[i];
          if (dead) o = f4{__builtin_nanf(""), __builtin_nanf(""), __builtin_nanf(""), __builtin_nanf("")};
          const v2u ob = {pack_bf16(o[0], o[1]), pack_bf16(o[2], o[3])};
          if (pass == 0) __builtin_amdgcn_raw_buffer_store_b64(ob, rws, OFF_X2 + (td * 4 + fb) * 32 + fcg * 8, 0, AUX_SC1);
          else *(v2u*)(a.out + fb * D + td * 16 + fcg * 4) = ob;
        }
      }
      __syncthreads();
    }
    if (pass == 0) {
      int off[3];
#pragma unroll
      for (int i = 0; i < 3; ++i) {
        const int s = w + 8 * i;
        off[i] = (c < 4 && s < 20) ? OFF_X2 + ((2 * s + (g >> 1)) * 4 + c) * 32 + (g & 1) * 16 : OOB;
      }
      gather<3>(xf, rws, off, dead);
      STAMP(6);
      q_phase<1>(L, xf, wqf, csq, rws, tq, w, l);
      STAMP(7);
    }
  }
}

__global__ __launch_bounds__(512, 2) void mega_kernel(const u16* __restrict__ x, const u16* __restrict__ K, const u16* __restrict__ V,
                                                      const u16* __restrict__ wq, const u16* __restrict__ wo, const u16* __restrict__ wg,
                                                      const u16* __restrict__ wu, const u16* __restrict__ wd, u16* __restrict__ out,
                                                      unsigned char* __restrict__ ws) {
  __shared__ __attribute__((aligned(16))) unsigned char lds[L_END];
  LAS unsigned char* L = (LAS unsigned char*)lds;
  const int cu = blockIdx.x, xc = cu & 7, y = cu >> 3;
  const int l = threadIdx.x & 63, w = __builtin_amdgcn_readfirstlane(threadIdx.x >> 6);
  const rsrc_t rws = mkrsrc(ws, WS_USED);
  unsigned long long st[16];
  STAMP(0);
  if (y < 16) attn_role(L, K, V, rws, xc, y, w, l, st);
  else {
    WArgs a{x, wq, wo, wg, wu, wd, out};
    weight_role(L, a, rws, xc, y - 16, w, l, st);
  }
}
}

extern "C" void kernel_launch(void* const* d_in, const int* in_sizes, int n_in, void* d_out, int out_size, void* d_ws, size_t ws_size,
                              hipStream_t stream) {
  (void)in_sizes; (void)n_in; (void)out_size; (void)ws_size;
  hipMemsetAsync(d_ws, 0xFF, WS_USED, stream);
  mega_kernel<<<256, 512, 0, stream>>>((const u16*)d_in[0], (const u16*)d_in[1], (const u16*)d_in[2], (const u16*)d_in[3],
                                       (const u16*)d_in[4], (const u16*)d_in[6], (const u16*)d_in[7], (const u16*)d_in[8],
                                       (u16*)d_out, (unsigned char*)d_ws);
}
```

```cpp
#include <hip/hip_runtime.h>
#include <stdint.h>

#define MK_STAMPS 0
#define HOLD_T 1100

namespace {
typedef unsigned short u16;
typedef unsigned v4u __attribute__((ext_vector_type(4)));
typedef unsigned v2u __attribute__((ext_vector_type(2)));
typedef float f4 __attribute__((ext_vector_type(4)));
typedef float f2 __attribute__((ext_vector_type(2)));
typedef __bf16 bf8 __attribute__((ext_vector_type(8)));
typedef __bf16 bf2 __attribute__((ext_vector_type(2)));
typedef short s4 __attribute__((ext_vector_type(4)));
typedef __amdgpu_buffer_rsrc_t rsrc_t;
#define LAS __attribute__((address_space(3)))

constexpr int NB = 4, D = 640, NH = 32, DH = 128, SK = 4096, WIN = 1024, FF = 1728, HD = 4096;
constexpr unsigned SENT = 0xFFFFFFFFu;
constexpr int OOB = 0x40000000;
constexpr int SZ_Q = 32 * 4 * 256, SZ_O = 32 * 4 * 256, SZ_X1 = 4 * 40 * 128, SZ_H = 108 * 128, SZ_X2 = 40 * 128;
constexpr int OFF_Q = 0, OFF_O = OFF_Q + 2 * SZ_Q, OFF_X1 = OFF_O + 2 * SZ_O, OFF_H = OFF_X1 + 2 * SZ_X1,
              OFF_X2 = OFF_H + 2 * SZ_H, OFF_FLAG = OFF_X2 + SZ_X2, OFF_EP = OFF_FLAG + 2048, WS_USED = OFF_EP + 128;
constexpr int OFF_STAMP = 1 << 20;
constexpr int SPIN_CAP = 1 << 17;
constexpr int L_V = 0;
constexpr int L_P = 131072;
constexpr int L_OW = L_P + 2048;
constexpr int L_ML = L_OW + 4096;
constexpr int L_Q = L_ML + 64;
constexpr int L_QF = L_Q + 256;
constexpr int L_END = L_QF + 16;
constexpr int L_RED = 0;
constexpr int RED_W = 5 * 36 * 4;

constexpr int AUX_NT = 2, AUX_SC1 = 16;

__device__ __forceinline__ f4 mfma(v4u a, v4u b, f4 c) {
  return __builtin_amdgcn_mfma_f32_16x16x32_bf16(__builtin_bit_cast(bf8, a), __builtin_bit_cast(bf8, b), c, 0, 0, 0);
}
__device__ __forceinline__ unsigned hi16pack(float hi, float lo) {
  return __builtin_amdgcn_perm(__builtin_bit_cast(unsigned, hi), __builtin_bit_cast(unsigned, lo), 0x07060302u);
}
__device__ __forceinline__ unsigned pack_bf16(float lo, float hi) {
  bf2 r = {(__bf16)lo, (__bf16)hi};
  unsigned u = __builtin_bit_cast(unsigned, r);
  return u == SENT ? 0x7FFF7FFFu : u;
}
__device__ __forceinline__ float bflo(unsigned u) { return __builtin_bit_cast(float, u << 16); }
__device__ __forceinline__ float bfhi(unsigned u) { return __builtin_bit_cast(float, u & 0xFFFF0000u); }
__device__ __forceinline__ float dot2(unsigned a, unsigned b, float c) {
  return __builtin_amdgcn_fdot2_f32_bf16(__builtin_bit_cast(bf2, a), __builtin_bit_cast(bf2, b), c, false);
}
template <int CTRL> __device__ __forceinline__ float dpp(float x) {
  return __builtin_bit_cast(float, __builtin_amdgcn_mov_dpp(__builtin_bit_cast(int, x), CTRL, 0xf, 0xf, true));
}
constexpr int XOR1 = 0xB1, XOR2 = 0x4E, ROR4 = 0x124, ROR8 = 0x128, SHL4 = 0x104;
__device__ __forceinline__ float xrow_sum(float x) {
  auto s = __builtin_amdgcn_permlane16_swap(__float_as_uint(x), __float_as_uint(x), false, false);
  x = __uint_as_float(s[0]) + __uint_as_float(s[1]);
  auto t = __builtin_amdgcn_permlane32_swap(__float_as_uint(x), __float_as_uint(x), false, false);
  return __uint_as_float(t[0]) + __uint_as_float(t[1]);
}
__device__ __forceinline__ float xrow_max(float x) {
  auto s = __builtin_amdgcn_permlane16_swap(__float_as_uint(x), __float_as_uint(x), false, false);
  x = fmaxf(__uint_as_float(s[0]), __uint_as_float(s[1]));
  auto t = __builtin_amdgcn_permlane32_swap(__float_as_uint(x), __float_as_uint(x), false, false);
  return fmaxf(__uint_as_float(t[0]), __uint_as_float(t[1]));
}
__device__ __forceinline__ float x32_sum(float x) {
  auto t = __builtin_amdgcn_permlane32_swap(__float_as_uint(x), __float_as_uint(x), false, false);
  return __uint_as_float(t[0]) + __uint_as_float(t[1]);
}
__device__ __forceinline__ rsrc_t mkrsrc(const void* p, unsigned bytes) {
  return __builtin_amdgcn_make_buffer_rsrc((void*)p, 0, bytes, 0x00020000);
}
template <int AUX> __device__ __forceinline__ v4u ld128(rsrc_t rs, int voff, int soff) {
  return __builtin_amdgcn_raw_buffer_load_b128(rs, voff, soff, AUX);
}
__device__ __forceinline__ unsigned umax3(unsigned a, unsigned b, unsigned c) { return max(max(a, b), c); }
__device__ __forceinline__ unsigned vmax4(v4u x, unsigned m) { return umax3(x.x, x.y, umax3(x.z, x.w, m)); }

#define DLY 12
#define SETTLE() __builtin_amdgcn_s_sleep(DLY)
template <int N> __device__ __forceinline__ void gather(v4u (&x)[N], rsrc_t rs, const int (&off)[N], bool& dead) {
  for (int spin = 0;; ++spin) {
    asm volatile("" ::: "memory");
#pragma unroll
    for (int i = 0; i < N; ++i) x[i] = ld128<AUX_SC1>(rs, off[i], 0);
    unsigned m = 0u;
#pragma unroll
    for (int i = 0; i < N; ++i) m = vmax4(x[i], m);
    if (!__any(m == SENT)) break;
    if (dead || spin > SPIN_CAP) { dead = true; break; }
    __builtin_amdgcn_s_sleep(2);
  }
}
__device__ __forceinline__ unsigned long long epoch_load(rsrc_t rws) {
  const v2u e = __builtin_amdgcn_raw_buffer_load_b64(rws, OFF_EP, 0, AUX_SC1);
  return ((unsigned long long)(unsigned)__builtin_amdgcn_readfirstlane(e.y) << 32 | (unsigned)__builtin_amdgcn_readfirstlane(e.x)) + 1ull;
}
__device__ __forceinline__ void clear16(rsrc_t rws, int off) {
  __builtin_amdgcn_raw_buffer_store_b128(v4u{SENT, SENT, SENT, SENT}, rws, off, 0, AUX_SC1);
}
__device__ __forceinline__ void flag_post(rsrc_t rws, int cu, unsigned long long tag, int l) {
  asm volatile("s_waitcnt vmcnt(0)" ::: "memory");
  if (l == 0) __builtin_amdgcn_raw_buffer_store_b64(v2u{(unsigned)tag, (unsigned)(tag >> 32)}, rws, OFF_FLAG + cu * 8, 0, AUX_SC1);
}
__device__ __forceinline__ void flags_wait(rsrc_t rws, unsigned long long tag, int l, bool& dead) {
  const unsigned lo = (unsigned)tag, hi = (unsigned)(tag >> 32);
  for (int spin = 0;; ++spin) {
    asm volatile("" ::: "memory");
    const v4u a = ld128<AUX_SC1>(rws, OFF_FLAG + l * 32, 0), b = ld128<AUX_SC1>(rws, OFF_FLAG + l * 32 + 16, 0);
    const bool ok = (a.x == lo) & (a.y == hi) & (a.z == lo) & (a.w == hi) & (b.x == lo) & (b.y == hi) & (b.z == lo) & (b.w == hi);
    if (__all(ok)) break;
    if (dead || spin > SPIN_CAP) { dead = true; break; }
    __builtin_amdgcn_s_sleep(4);
  }
}
__device__ __forceinline__ v4u xpose16(v4u raw, v4u sel1, v4u sel2) {
  const f4 z = {0.f, 0.f, 0.f, 0.f};
  const f4 d1 = mfma(raw, sel1, z), d2 = mfma(raw, sel2, z);
  return v4u{hi16pack(d1[1], d1[0]), hi16pack(d1[3], d1[2]), hi16pack(d2[1], d2[0]), hi16pack(d2[3], d2[2])};
}
__device__ __forceinline__ void xpose32(v4u& a, v4u& b, v4u sel1, v4u sel2) {
  const f4 z = {0.f, 0.f, 0.f, 0.f};
  const f4 a1 = mfma(a, sel1, z), b1 = mfma(b, sel1, z), a2 = mfma(a, sel2, z), b2 = mfma(b, sel2, z);
  a = v4u{hi16pack(a1[1], a1[0]), hi16pack(a1[3], a1[2]), hi16pack(b1[1], b1[0]), hi16pack(b1[3], b1[2])};
  b = v4u{hi16pack(a2[1], a2[0]), hi16pack(a2[3], a2[2]), hi16pack(b2[1], b2[0]), hi16pack(b2[3], b2[2])};
}

#define STAMP(i) do { } while (0)

#define ASM_LD(dst, voff, rs, soff, MODS) \
  asm volatile("buffer_load_dwordx4 %0, %1, %2, %3 offen " MODS : "=v"(dst) : "v"(voff), "s"(rs), "s"(soff) : "memory")
#define ASM_DMA(voff, rs, soff, ldsa) \
  asm volatile("s_mov_b32 m0, %3\n\ts_nop 0\n\tbuffer_load_dwordx4 %0, %1, %2 offen nt lds" ::"v"(voff), "s"(rs), "s"(soff), "s"(ldsa) : "memory")
#define WAIT_VM(n) asm volatile("s_waitcnt vmcnt(" #n ")" ::: "memory")
#define PIN8(a, o) asm volatile("" : "+v"(a[o]), "+v"(a[o + 1]), "+v"(a[o + 2]), "+v"(a[o + 3]), "+v"(a[o + 4]), "+v"(a[o + 5]), "+v"(a[o + 6]), "+v"(a[o + 7]))

__device__ __forceinline__ v2u tr_read(LAS unsigned char* p) {
  s4 t = __builtin_amdgcn_ds_read_tr16_b64_v4i16((LAS s4*)p);
  return __builtin_bit_cast(v2u, t);
}

template <int PASS>
__device__ __forceinline__ void attn_math(LAS unsigned char* L, const v4u (&kf)[32], const v4u (&vf)[16], const v4u (&qf)[4], rsrc_t rws,
                                          int b, int h, int w, int l, unsigned long long (&st)[16]) {
  const int g = l >> 4;
  const f4 z = {0.f, 0.f, 0.f, 0.f};
  const v4u ones = {0x3F803F80u, 0x3F803F80u, 0x3F803F80u, 0x3F803F80u};
  constexpr float CS = 0.08838834764831845f * 1.4426950408889634f;
  float s0 = 0.f, s1 = 0.f;
#pragma unroll
  for (int t = 0; t < 8; ++t) {
    f4 d = z;
#pragma unroll
    for (int s = 0; s < 4; ++s) d = mfma(qf[s], kf[t * 4 + s], d);
    if (t < 4) s0 = (g == t) ? d[0] : s0; else s1 = (g == t - 4) ? d[0] : s1;
  }
  float m = fmaxf(s0, s1);
  m = fmaxf(m, dpp<XOR1>(m)); m = fmaxf(m, dpp<XOR2>(m)); m = fmaxf(m, dpp<ROR4>(m)); m = fmaxf(m, dpp<ROR8>(m));
  m = xrow_max(m);
  const float p0 = __builtin_amdgcn_exp2f((s0 - m) * CS), p1 = __builtin_amdgcn_exp2f((s1 - m) * CS);
  LAS __bf16* pw = (LAS __bf16*)(L + L_P + w * 256);
  pw[l] = (__bf16)p0; pw[64 + l] = (__bf16)p1;
  v4u pf[4];
#pragma unroll
  for (int s = 0; s < 4; ++s) pf[s] = *(LAS v4u*)(L + L_P + w * 256 + s * 64 + g * 16);
  float oa0 = 0.f, oa1 = 0.f;
#pragma unroll
  for (int n = 0; n < 8; ++n) {
    f4 d = mfma(pf[0], vf[n], z);
    d = mfma(pf[1], vf[8 + n], d);
    if (n < 4) oa0 = (g == n) ? d[0] : oa0; else oa1 = (g == n - 4) ? d[0] : oa1;
  }
  f4 dl = z;
#pragma unroll
  for (int s = 0; s < 4; ++s) dl = mfma(pf[s], ones, dl);
  if (PASS == 0) WAIT_VM(0);
  STAMP(PASS == 0 ? 2 : 6);
  const int q4 = (l >> 2) & 3, p4 = l & 3;
  const int trb = L_V + w * 16384 + (8 * g + q4) * 256 + ((q4 | ((g & 1) << 2)) << 5) + p4 * 8;
  float ob0 = 0.f, ob1 = 0.f;
#pragma unroll
  for (int n = 0; n < 8; ++n) {
    LAS unsigned char* a = L + (trb ^ (n << 5));
    const v2u t0 = tr_read(a), t1 = tr_read(a + 1024), t2 = tr_read(a + 8192), t3 = tr_read(a + 8192 + 1024);
    f4 d = mfma(pf[2], v4u{t0.x, t0.y, t1.x, t1.y}, z);
    d = mfma(pf[3], v4u{t2.x, t2.y, t3.x, t3.y}, d);
    if (n < 4) ob0 = (g == n) ? d[0] : ob0; else ob1 = (g == n - 4) ? d[0] : ob1;
  }
  LAS float* ow = (LAS float*)(L + L_OW);
  ow[w * 128 + l] = oa0 + ob0; ow[w * 128 + 64 + l] = oa1 + ob1;
  if (l == 0) *(LAS f2*)(L + L_ML + w * 8) = f2{m, dl[0]};
  __syncthreads();
  if (w == 0) {
    f2 ml[8], ov[8];
#pragma unroll
    for (int i = 0; i < 8; ++i) { ml[i] = *(LAS f2*)(L + L_ML + i * 8); ov[i] = *(LAS f2*)(L + L_OW + i * 512 + l * 8); }
    float M = ml[0].x;
#pragma unroll
    for (int i = 1; i < 8; ++i) M = fmaxf(M, ml[i].x);
    float Ls = 0.f, o0 = 0.f, o1 = 0.f;
#pragma unroll
    for (int i = 0; i < 8; ++i) {
      const float f = __builtin_amdgcn_exp2f((ml[i].x - M) * CS);
      Ls = fmaf(f, ml[i].y, Ls); o0 = fmaf(f, ov[i].x, o0); o1 = fmaf(f, ov[i].y, o1);
    }
    const float inv = 1.0f / Ls;
    __builtin_amdgcn_raw_buffer_store_b32(pack_bf16(o0 * inv, o1 * inv), rws, OFF_O + PASS * SZ_O + (h * 4 + b) * 256 + l * 4, 0, AUX_SC1);
  }
  STAMP(PASS == 0 ? 3 : 7);
}

__device__ __forceinline__ void attn_role(LAS unsigned char* L, const u16* K, const u16* V, rsrc_t rws, int cu, int xc, int y, int w, int l,
                                          unsigned long long (&st)[16]) {
  const int h = 4 * xc + (y >> 2), b = y & 3, g = l >> 4, r = l & 15;
  const size_t kv = ((size_t)(b * NH + h) * SK + (SK - WIN) + 128 * w) * DH;
  const rsrc_t rK = mkrsrc(K + kv, 32768), rV = mkrsrc(V + kv, 32768);
  constexpr int PSTR = 8192;
  bool dead = false;
  if (threadIdx.x == 0) *(volatile LAS unsigned*)(L + L_QF) = 0u;
  asm volatile("s_waitcnt lgkmcnt(0)\n\ts_barrier" ::: "memory");
  v2u ep = {0u, 0u};
  unsigned long long tag = 0;
  asm volatile("buffer_load_dwordx2 %0, %1, %2, 0 offen sc1" : "=v"(ep) : "v"(w == 0 ? OFF_EP : OOB), "s"(rws) : "memory");
  {
    const v4u sent = {SENT, SENT, SENT, SENT};
    const int co = (w == 0 && l < 32) ? OFF_O + (l >> 4) * SZ_O + (h * 4 + b) * 256 + r * 16 : OOB;
    asm volatile("buffer_store_dwordx4 %0, %1, %2, 0 offen sc1\n\ts_nop 1" ::"v"(sent), "v"(co), "s"(rws) : "memory");
  }
#define SVC_AT 3
#define SVC(n) do { if (SVC_AT == (n) && w == 0) { \
      flags_wait(rws, tag, l, dead); \
      v4u q1[1]; const int off[1] = {l < 16 ? OFF_Q + (h * 16 + b) * 64 + (l >> 2) * 256 + (l & 3) * 16 : OOB}; \
      gather<1>(q1, rws, off, dead); \
      if (l < 16) *(LAS v4u*)(L + L_Q + l * 16) = q1[0]; \
      asm volatile("s_waitcnt lgkmcnt(0)" ::: "memory"); \
      if (l == 0) *(volatile LAS unsigned*)(L + L_QF) = 1u; } } while (0)
  v4u kf[32], vf[16], qf[4];
  const int ldsV = L_V + w * 16384;
  const int ks0 = g * 256 + ((r ^ g) << 4);
#define KX(i) (((i) & 3) << 6)
  const int vs0 = g * 256 + (((r >> 1) ^ g) << 5) + (l & 1) * 16, vs1 = g * 256 + (((r >> 1) ^ (g | 4)) << 5) + (l & 1) * 16;
#define K_PIECE(n) _Pragma("unroll") for (int i = 0; i < 8; ++i) \
    ASM_DMA(ks0 ^ KX(i), rK, (n) * PSTR + i * 1024, ldsV + ((n) & 1) * 8192 + i * 1024)
#define V_PIECE(n, buf) _Pragma("unroll") for (int i = 0; i < 8; ++i) \
    ASM_DMA((i & 2) ? vs1 : vs0, rV, (n) * PSTR + i * 1024, ldsV + (buf) * 8192 + i * 1024)
#define K_READ(n) do { _Pragma("unroll") for (int t = 0; t < 2; ++t) _Pragma("unroll") for (int s = 0; s < 4; ++s) \
      kf[((n) * 2 + t) * 4 + s] = *(LAS v4u*)(L + ldsV + ((n) & 1) * 8192 + (16 * t + r) * 256 + (((4 * s + g) ^ r) << 4)); \
    asm volatile("s_waitcnt lgkmcnt(0)" ::: "memory"); PIN8(kf, (n) * 8); } while (0)
  const int trb = ldsV + (8 * g + ((l >> 2) & 3)) * 256 + ((((l >> 2) & 3) | ((g & 1) << 2)) << 5) + (l & 3) * 8;
#define V_READ(n) do { _Pragma("unroll") for (int c8 = 0; c8 < 8; ++c8) { \
      LAS unsigned char* a = L + (trb ^ (c8 << 5)) + (n) * 8192; \
      const v2u t0 = tr_read(a), t1 = tr_read(a + 1024); \
      vf[(n) * 8 + c8] = v4u{t0.x, t0.y, t1.x, t1.y}; } \
    asm volatile("s_waitcnt lgkmcnt(0)" ::: "memory"); PIN8(vf, (n) * 8); } while (0)
  K_PIECE(0); K_PIECE(1);
  asm volatile("s_waitcnt vmcnt(16)" : "+v"(ep)::"memory");
  tag = ((unsigned long long)(unsigned)__builtin_amdgcn_readfirstlane(ep.y) << 32 | (unsigned)__builtin_amdgcn_readfirstlane(ep.x)) + 1ull;
  {
    const v2u tg = {(unsigned)tag, (unsigned)(tag >> 32)};
    asm volatile("buffer_store_dwordx2 %0, %1, %2, 0 offen sc1" ::"v"(tg), "v"((w == 0 && l == 0) ? OFF_FLAG + cu * 8 : OOB), "s"(rws) : "memory");
  }
  WAIT_VM(9);
  K_READ(0); K_PIECE(2);
  WAIT_VM(8); K_READ(1); K_PIECE(3); SVC(1);
  WAIT_VM(8); K_READ(2); V_PIECE(0, 0); SVC(2);
  WAIT_VM(8); K_READ(3); V_PIECE(1, 1); SVC(3);
  WAIT_VM(8); V_READ(0); V_PIECE(2, 0); SVC(4);
  WAIT_VM(8); V_READ(1); V_PIECE(3, 1); SVC(5);
  STAMP(1);
  for (int spin = 0; *(volatile LAS unsigned*)(L + L_QF) == 0u; ++spin) {
    if (spin > (SPIN_CAP << 4)) { dead = true; break; }
    __builtin_amdgcn_s_sleep(1);
  }
  STAMP(4);
#pragma unroll
  for (int s = 0; s < 4; ++s) qf[s] = *(LAS v4u*)(L + L_Q + s * 64 + g * 16);
  attn_math<0>(L, kf, vf, qf, rws, b, h, w, l, st);
  {
    const int qo = OFF_Q + SZ_Q + (h * 16 + b) * 64 + g * 16;
    const int off[4] = {qo, qo + 256, qo + 512, qo + 768};
    gather<4>(qf, rws, off, dead);
  }
  STAMP(5);
  attn_math<1>(L, kf, vf, qf, rws, b, h, w, l, st);
}

__device__ __forceinline__ f4 red_sum(LAS unsigned char* L, int row, int l) {
  const int hf = l >> 5, cg = l & 7;
  f4 s = *(LAS f4*)(L + L_RED + (hf * 4) * RED_W + row * 144 + cg * 16);
#pragma unroll
  for (int i = 1; i < 4; ++i) s += *(LAS f4*)(L + L_RED + (hf * 4 + i) * RED_W + row * 144 + cg * 16);
  return f4{x32_sum(s[0]), x32_sum(s[1]), x32_sum(s[2]), x32_sum(s[3])};
}
__device__ __forceinline__ f2 red_stat(LAS unsigned char* L, int row, int l) {
  const int hf = l >> 5;
  f2 s = *(LAS f2*)(L + L_RED + (hf * 4) * RED_W + row * 144 + 128);
#pragma unroll
  for (int i = 1; i < 4; ++i) s += *(LAS f2*)(L + L_RED + (hf * 4 + i) * RED_W + row * 144 + 128);
  return f2{x32_sum(s[0]), x32_sum(s[1])};
}

__device__ __forceinline__ f4 red_sum_gu(LAS unsigned char* L, int row, int l) {
  const int m = l >> 5, cg = l & 7;
  f4 s = *(LAS f4*)(L + L_RED + m * RED_W + row * 144 + cg * 16);
#pragma unroll
  for (int i = 1; i < 4; ++i) s += *(LAS f4*)(L + L_RED + (2 * i + m) * RED_W + row * 144 + cg * 16);
  return s;
}
struct WArgs { const u16 *x, *wq, *wo, *wg, *wu, *wd; u16* out; };

template <int PASS>
__device__ __forceinline__ void q_phase(LAS unsigned char* L, const v4u (&xf)[3], const v4u (&wqf)[6], f4 csq, rsrc_t rws, int tq, int w, int l) {
  const int g = l >> 4, c = l & 15;
  const f4 z = {0.f, 0.f, 0.f, 0.f};
  const v4u ones = {0x3F803F80u, 0x3F803F80u, 0x3F803F80u, 0x3F803F80u};
  f4 d0 = z, d1 = z, ds = z;
  float ss = 0.f;
#pragma unroll
  for (int i = 0; i < 3; ++i) {
    d0 = mfma(wqf[2 * i], xf[i], d0); d1 = mfma(wqf[2 * i + 1], xf[i], d1); ds = mfma(ones, xf[i], ds);
    ss = dot2(xf[i].x, xf[i].x, ss); ss = dot2(xf[i].y, xf[i].y, ss); ss = dot2(xf[i].z, xf[i].z, ss); ss = dot2(xf[i].w, xf[i].w, ss);
  }
  ss = xrow_sum(ss);
  LAS unsigned char* R = L + L_RED + w * RED_W + c * 144;
  if (c < 4) {
    *(LAS f4*)(R + g * 16) = d0; *(LAS f4*)(R + 64 + g * 16) = d1;
    if (g == 0) *(LAS f2*)(R + 128) = f2{ds[0], ss};
  }
  __syncthreads();
  if (w == 0) {
    const int b = (l >> 3) & 3, cg = l & 7;
    const f4 p = red_sum(L, b, l);
    const f2 s = red_stat(L, b, l);
    const float mu = s[0] * (1.0f / D), var = fmaxf(s[1] * (1.0f / D) - mu * mu, 0.f), rs = __builtin_amdgcn_rsqf(var + 1e-5f);
    if (l < 32) {
      const v2u o = {pack_bf16((p[0] - mu * csq[0]) * rs, (p[1] - mu * csq[1]) * rs), pack_bf16((p[2] - mu * csq[2]) * rs, (p[3] - mu * csq[3]) * rs)};
      __builtin_amdgcn_raw_buffer_store_b64(o, rws, OFF_Q + PASS * SZ_Q + tq * 256 + b * 64 + cg * 8, 0, AUX_SC1);
    }
  }
  __syncthreads();
}

__device__ __forceinline__ void q_second(LAS unsigned char* L, const v4u (&wqf)[6], f4 csq, rsrc_t rws, int tq, int w, int l, bool& dead,
                                         unsigned long long (&st)[16]) {
  const int g = l >> 4, c = l & 15;
  v4u xf[3];
  int off[3];
#pragma unroll
  for (int i = 0; i < 3; ++i) {
    const int s = w + 8 * i;
    off[i] = (c < 4 && s < 20) ? OFF_X2 + ((2 * s + (g >> 1)) * 4 + c) * 32 + (g & 1) * 16 : OOB;
  }
  gather<3>(xf, rws, off, dead);
  STAMP(6);
  q_phase<1>(L, xf, wqf, csq, rws, tq, w, l);
  STAMP(7);
}

__device__ __forceinline__ void weight_role(LAS unsigned char* L, const WArgs& a, rsrc_t rws, int cu, int xc, int u, int w, int l,
                                            unsigned long long (&st)[16]) {
  const int g = l >> 4, r = l & 15, c = r;
  const f4 z = {0.f, 0.f, 0.f, 0.f};
  const v4u ones = {0x3F803F80u, 0x3F803F80u, 0x3F803F80u, 0x3F803F80u};
  v4u sel1, sel2;
  {
    const int j = r - 8 * (g & 1);
    const unsigned one = (j & 1) ? 0x3F800000u : 0x00003F80u;
    const bool in = (unsigned)j < 8u;
    v4u s = {(in && (j >> 1) == 0) ? one : 0u, (in && (j >> 1) == 1) ? one : 0u, (in && (j >> 1) == 2) ? one : 0u, (in && (j >> 1) == 3) ? one : 0u};
    const v4u zz = {0u, 0u, 0u, 0u};
    sel1 = g < 2 ? s : zz; sel2 = g < 2 ? zz : s;
  }
  const int tq = xc * 16 + u;
  const bool has_wo = u < 10, has_gu = u < (xc < 3 ? 8 : 6), has_dn = u >= 10 && u < 15;
  const int wo_id = xc * 10 + u, kq = wo_id / 20, nt32 = wo_id % 20;
  const int t32 = xc < 3 ? xc * 8 + u : 24 + (xc - 3) * 6 + u;
  const int td = xc * 5 + (u - 10);
  const int rho16 = 8 * (r >> 2) + (r & 3);
  const int rho32 = rho16 + 4 * (g >> 1);

  bool dead = false;
  const unsigned long long tstart = __builtin_amdgcn_s_memrealtime();
  v2u ep = {0u, 0u};
  if (w == 0) {
    ep = __builtin_amdgcn_raw_buffer_load_b64(rws, OFF_EP, 0, AUX_SC1);
    const int hf = l >> 5, k = l & 31;
    clear16(rws, k < 16 ? OFF_Q + hf * SZ_Q + tq * 256 + k * 16 : has_wo ? OFF_X1 + hf * SZ_X1 + (kq * 40 + 2 * nt32) * 128 + (k - 16) * 16 : OOB);
    clear16(rws, (k < 16 && has_gu) ? OFF_H + hf * SZ_H + t32 * 256 + k * 16 : (k < 8 && hf == 0 && has_dn) ? OFF_X2 + td * 128 + k * 16 : OOB);
  }
  v4u wqf[6];
  unsigned long long tag = 0;
  f4 csq;
  {
    v4u xf[3];
    const rsrc_t rx = mkrsrc(a.x, NB * D * 2), rq = mkrsrc(a.wq, D * HD * 2);
#pragma unroll
    for (int i = 0; i < 3; ++i) {
      const int s = w + 8 * i;
      xf[i] = ld128<0>(rx, (c < 4 && s < 20) ? c * 1280 + s * 64 + g * 16 : OOB, 0);
    }
#pragma unroll
    for (int i = 0; i < 3; ++i) {
      const int s = w + 8 * i;
      const int vo = s < 20 ? (32 * s + rho16) * 8192 + tq * 64 + g * 16 : OOB;
      wqf[2 * i] = ld128<AUX_NT>(rq, vo, 0); wqf[2 * i + 1] = ld128<AUX_NT>(rq, vo, 4 * 8192);
    }
    __builtin_amdgcn_sched_barrier(0);
    if (w == 0) {
      asm volatile("s_waitcnt vmcnt(9)" : "+v"(ep)::"memory");
      tag = ((unsigned long long)(unsigned)__builtin_amdgcn_readfirstlane(ep.y) << 32 | (unsigned)__builtin_amdgcn_readfirstlane(ep.x)) + 1ull;
      if (l == 0) __builtin_amdgcn_raw_buffer_store_b64(v2u{(unsigned)tag, (unsigned)(tag >> 32)}, rws, OFF_FLAG + cu * 8, 0, AUX_SC1);
    }
#pragma unroll
    for (int i = 0; i < 3; ++i) xpose32(wqf[2 * i], wqf[2 * i + 1], sel1, sel2);
    f4 c0 = z, c1 = z;
#pragma unroll
    for (int i = 0; i < 3; ++i) { c0 = mfma(wqf[2 * i], ones, c0); c1 = mfma(wqf[2 * i + 1], ones, c1); }
    LAS unsigned char* R4 = L + L_RED + w * RED_W + 4 * 144;
    if (c == 0) { *(LAS f4*)(R4 + g * 16) = c0; *(LAS f4*)(R4 + 64 + g * 16) = c1; }
    __syncthreads();
    csq = red_sum(L, 4, l);
    q_phase<0>(L, xf, wqf, csq, rws, tq, w, l);
  }
  STAMP(1);
  const int fb = (l >> 3) & 3, fcg = l & 7;
  LAS unsigned char* R = L + L_RED + w * RED_W + c * 144;
#define HOLD() do { while ((long long)(__builtin_amdgcn_s_memrealtime() - tstart) < HOLD_T) __builtin_amdgcn_s_sleep(16); } while (0)

  if (has_wo) {
    v4u wof[8], guf[10];
    const int gm = w & 1, gq = w >> 1;
    {
      const rsrc_t ro = mkrsrc(a.wo, D * HD * 2), rgu = mkrsrc(gm ? a.wu : a.wg, D * FF * 2);
      const int vo = ((8 * kq + w) * 128 + rho16) * 1280 + nt32 * 64 + g * 16;
#pragma unroll
      for (int s = 0; s < 4; ++s) { wof[2 * s] = ld128<AUX_NT>(ro, vo, s * 32 * 1280); wof[2 * s + 1] = ld128<AUX_NT>(ro, vo, (s * 32 + 4) * 1280); }
      const int vg = has_gu ? (160 * gq + rho16) * 3456 + t32 * 64 + g * 16 : OOB;
#pragma unroll
      for (int i = 0; i < 5; ++i) { guf[2 * i] = ld128<AUX_NT>(rgu, vg, i * 32 * 3456); guf[2 * i + 1] = ld128<AUX_NT>(rgu, vg, (i * 32 + 4) * 3456); }
      __builtin_amdgcn_sched_barrier(0);
    }
#pragma unroll
    for (int s = 0; s < 4; ++s) xpose32(wof[2 * s], wof[2 * s + 1], sel1, sel2);
#pragma unroll
    for (int i = 0; i < 5; ++i) xpose32(guf[2 * i], guf[2 * i + 1], sel1, sel2);
    f4 csg;
    {
      f4 c0 = z, c1 = z;
#pragma unroll
      for (int i = 0; i < 5; ++i) { c0 = mfma(guf[2 * i], ones, c0); c1 = mfma(guf[2 * i + 1], ones, c1); }
      LAS unsigned char* R4 = L + L_RED + w * RED_W + 4 * 144;
      if (c == 0) { *(LAS f4*)(R4 + g * 16) = c0; *(LAS f4*)(R4 + 64 + g * 16) = c1; }
      if (w == 0) flags_wait(rws, tag, l, dead);
      __syncthreads();
      csg = red_sum_gu(L, 4, l);
    }
    STAMP(2);
    HOLD();
#pragma unroll
    for (int pass = 0; pass < 2; ++pass) {
      {
        v4u of[4];
        int lo = l; asm volatile("" : "+v"(lo));
        const int o0 = (lo & 15) < 4 ? OFF_O + pass * SZ_O + ((8 * kq + w) * 4 + (lo & 15)) * 256 + (lo >> 4) * 16 : OOB;
        const int off[4] = {o0, o0 + 64, o0 + 128, o0 + 192};
        v2u res = {0u, 0u};
        if (w == 0 && kq == 0 && l < 32) {
          if (pass == 0) res = *(const v2u*)(a.x + fb * D + nt32 * 32 + fcg * 4);
          else res = __builtin_amdgcn_raw_buffer_load_b64(rws, OFF_X2 + ((2 * nt32 + (fcg >> 2)) * 4 + fb) * 32 + (fcg & 3) * 8, 0, AUX_SC1);
        }
        gather<4>(of, rws, off, dead);
        STAMP(pass == 0 ? 3 : 9);
        f4 d0 = z, d1 = z;
#pragma unroll
        for (int s = 0; s < 4; ++s) { d0 = mfma(wof[2 * s], of[s], d0); d1 = mfma(wof[2 * s + 1], of[s], d1); }
        if (c < 4) { *(LAS f4*)(R + g * 16) = d0; *(LAS f4*)(R + 64 + g * 16) = d1; }
        __syncthreads();
        if (w == 0) {
          f4 p = red_sum(L, fb, l);
          p[0] += bflo(res.x); p[1] += bfhi(res.x); p[2] += bflo(res.y); p[3] += bfhi(res.y);
          if (l < 32) {
            const v2u o = {pack_bf16(p[0], p[1]), pack_bf16(p[2], p[3])};
            __builtin_amdgcn_raw_buffer_store_b64(o, rws, OFF_X1 + pass * SZ_X1 + ((kq * 40 + 2 * nt32 + (fcg >> 2)) * 4 + fb) * 32 + (fcg & 3) * 8, 0, AUX_SC1);
          }
        }
        __syncthreads();
      }
      if (has_gu) {
        v4u x1[5];
        int xo[5];
        int lo = l; asm volatile("" : "+v"(lo));
#pragma unroll
        for (int i = 0; i < 5; ++i)
          xo[i] = OFF_X1 + pass * SZ_X1 + ((((lo & 15) >> 2) * 40 + 2 * (5 * gq + i) + (lo >> 5)) * 4 + (lo & 3)) * 32 + ((lo >> 4) & 1) * 16;
        SETTLE();
        gather<5>(x1, rws, xo, dead);
        STAMP(pass == 0 ? 4 : 10);
        f4 d0 = z, d1 = z, ds = z;
#pragma unroll
        for (int i = 0; i < 5; ++i) { d0 = mfma(guf[2 * i], x1[i], d0); d1 = mfma(guf[2 * i + 1], x1[i], d1); }
        if (gm == 0) {
#pragma unroll
          for (int i = 0; i < 5; ++i) ds = mfma(ones, x1[i], ds);
        }
#pragma unroll
        for (int i = 0; i < 4; ++i) {
          d0[i] += dpp<ROR4>(d0[i]); d0[i] += dpp<ROR8>(d0[i]);
          d1[i] += dpp<ROR4>(d1[i]); d1[i] += dpp<ROR8>(d1[i]);
        }
        float sx = ds[0]; sx += dpp<ROR4>(sx); sx += dpp<ROR8>(sx);
        if (c < 4) {
          *(LAS f4*)(R + g * 16) = d0; *(LAS f4*)(R + 64 + g * 16) = d1;
          if (g == 0) *(LAS float*)(R + 128) = sx;
        }
        __syncthreads();
        if (w == 0) {
          const f4 p = red_sum_gu(L, fb, l);
          float sm = 0.f;
#pragma unroll
          for (int i = 0; i < 4; ++i) sm += *(LAS float*)(L + L_RED + 2 * i * RED_W + fb * 144 + 128);
          const float mu = sm * (1.0f / D);
          float hv[4];
#pragma unroll
          for (int i = 0; i < 4; ++i) {
            const float v = p[i] - mu * csg[i];
            auto sw = __builtin_amdgcn_permlane32_swap(__float_as_uint(v), __float_as_uint(v), false, false);
            hv[i] = fmaxf(__uint_as_float(sw[0]), 0.f) * __uint_as_float(sw[1]);
          }
          if (l < 32) {
            const v2u o = {pack_bf16(hv[0], hv[1]), pack_bf16(hv[2], hv[3])};
            __builtin_amdgcn_raw_buffer_store_b64(o, rws, OFF_H + pass * SZ_H + ((2 * t32 + (fcg >> 2)) * 4 + fb) * 32 + (fcg & 3) * 8, 0, AUX_SC1);
          }
        }
        __syncthreads();
      }
      if (pass == 0) q_second(L, wqf, csq, rws, tq, w, l, dead, st);
    }
  } else {
    v4u wdf[7];
    {
      const rsrc_t rd = mkrsrc(a.wd, D * FF * 2);
#pragma unroll
      for (int i = 0; i < 7; ++i) {
        const int s = w + 8 * i;
        wdf[i] = ld128<AUX_NT>(rd, (has_dn && s < 54) ? (32 * s + rho32) * 1280 + td * 32 + (g & 1) * 16 : OOB, 0);
      }
      __builtin_amdgcn_sched_barrier(0);
    }
#pragma unroll
    for (int i = 0; i < 7; ++i) wdf[i] = xpose16(wdf[i], sel1, sel2);
    if (w == 0) flags_wait(rws, tag, l, dead);
    __syncthreads();
    STAMP(2);
    HOLD();
#pragma unroll
    for (int pass = 0; pass < 2; ++pass) {
      if (has_dn) {
        v4u x1[3];
        int xoff[3];
        int lo = l; asm volatile("" : "+v"(lo));
#pragma unroll
        for (int i = 0; i < 3; ++i) {
          const int s = w + 8 * i;
          xoff[i] = s < 20 ? OFF_X1 + pass * SZ_X1 + ((((lo & 15) >> 2) * 40 + 2 * s + (lo >> 5)) * 4 + (lo & 3)) * 32 + ((lo >> 4) & 1) * 16 : OOB;
        }
        gather<3>(x1, rws, xoff, dead);
        STAMP(pass == 0 ? 4 : 10);
        f4 gram = z, ds = z;
#pragma unroll
        for (int i = 0; i < 3; ++i) { gram = mfma(x1[i], x1[i], gram); ds = mfma(ones, x1[i], ds); }
        float sq = (c & 3) == 0 ? gram[0] : (c & 3) == 1 ? gram[1] : (c & 3) == 2 ? gram[2] : gram[3];
        sq += dpp<ROR4>(sq); sq += dpp<ROR8>(sq); sq = xrow_sum(sq);
        float sx = ds[0]; sx += dpp<ROR4>(sx); sx += dpp<ROR8>(sx);
        if (c < 4 && g == 0) *(LAS f2*)(R + 128) = f2{sx, sq};
        __syncthreads();
        float rs2 = 0.f; f4 res = z;
        if (w == 0) {
          v2u rp[4];
#pragma unroll
          for (int k = 0; k < 4; ++k)
            rp[k] = __builtin_amdgcn_raw_buffer_load_b64(rws, OFF_X1 + pass * SZ_X1 + ((k * 40 + td) * 4 + fb) * 32 + (fcg & 3) * 8, 0, AUX_SC1);
          const f2 s = red_stat(L, fb, l);
          const float mu = s[0] * (1.0f / D), var = fmaxf(s[1] * (1.0f / D) - mu * mu, 0.f);
          rs2 = 1.0f / (var + 1e-5f);
#pragma unroll
          for (int k = 0; k < 4; ++k) { res[0] += bflo(rp[k].x); res[1] += bfhi(rp[k].x); res[2] += bflo(rp[k].y); res[3] += bfhi(rp[k].y); }
        }
        v4u hf[7];
        int hoff[7];
#pragma unroll
        for (int i = 0; i < 7; ++i) {
          const int s = w + 8 * i;
          hoff[i] = ((lo & 15) < 4 && s < 54) ? OFF_H + pass * SZ_H + ((2 * s + (lo >> 5)) * 4 + (lo & 15)) * 32 + ((lo >> 4) & 1) * 16 : OOB;
        }
        gather<7>(hf, rws, hoff, dead);
        STAMP(pass == 0 ? 5 : 11);
        f4 d = z;
#pragma unroll
        for (int i = 0; i < 7; ++i) d = mfma(wdf[i], hf[i], d);
        if (c < 4) { *(LAS f4*)(R + g * 16) = d; *(LAS f4*)(R + 64 + g * 16) = z; }
        __syncthreads();
        if (w == 0) {
          const f4 p = red_sum(L, fb, l);
          if (l < 32 && fcg < 4) {
            f4 o;
#pragma unroll
            for (int i = 0; i < 4; ++i) o[i] = res[i] + rs2 * p[i];
            if (dead) o = f4{__builtin_nanf(""), __builtin_nanf(""), __builtin_nanf(""), __builtin_nanf("")};
            const v2u ob = {pack_bf16(o[0], o[1]), pack_bf16(o[2], o[3])};
            if (pass == 0) __builtin_amdgcn_raw_buffer_store_b64(ob, rws, OFF_X2 + (td * 4 + fb) * 32 + fcg * 8, 0, AUX_SC1);
            else *(v2u*)(a.out + fb * D + td * 16 + fcg * 4) = ob;
          }
          if (pass == 1 && td == 0 && l == 0) __builtin_amdgcn_raw_buffer_store_b64(v2u{(unsigned)tag, (unsigned)(tag >> 32)}, rws, OFF_EP, 0, AUX_SC1);
        }
        __syncthreads();
      }
      if (pass == 0) { if (has_dn) SETTLE(); q_second(L, wqf, csq, rws, tq, w, l, dead, st); }
    }
  }
}

__global__ __launch_bounds__(512, 2) void mega_kernel(const u16* __restrict__ x, const u16* __restrict__ K, const u16* __restrict__ V,
                                                      const u16* __restrict__ wq, const u16* __restrict__ wo, const u16* __restrict__ wg,
                                                      const u16* __restrict__ wu, const u16* __restrict__ wd, u16* __restrict__ out,
                                                      unsigned char* __restrict__ ws) {
  __shared__ __attribute__((aligned(16))) unsigned char lds[L_END];
  LAS unsigned char* L = (LAS unsigned char*)lds;
  const int cu = blockIdx.x, xc = cu & 7, y = cu >> 3;
  const int l = threadIdx.x & 63, w = __builtin_amdgcn_readfirstlane(threadIdx.x >> 6);
  const rsrc_t rws = mkrsrc(ws, WS_USED);
  unsigned long long st[16];
  STAMP(0);
  if (y < 16) attn_role(L, K, V, rws, cu, xc, y, w, l, st);
  else {
    WArgs a{x, wq, wo, wg, wu, wd, out};
    weight_role(L, a, rws, cu, xc, y - 16, w, l, st);
  }
}
}

extern "C" void kernel_launch(void* const* d_in, const int* in_sizes, int n_in, void* d_out, int out_size, void* d_ws, size_t ws_size,
                              hipStream_t stream) {
  (void)in_sizes; (void)n_in; (void)out_size; (void)ws_size;
  mega_kernel<<<256, 512, 0, stream>>>((const u16*)d_in[0], (const u16*)d_in[1], (const u16*)d_in[2], (const u16*)d_in[3],
                                       (const u16*)d_in[4], (const u16*)d_in[6], (const u16*)d_in[7], (const u16*)d_in[8],
                                       (u16*)d_out, (unsigned char*)d_ws);
}
```

```cpp
#include <hip/hip_runtime.h>
#include <stdint.h>

#define MK_STAMPS 0
#define HOLD_T 1100

namespace {
typedef unsigned short u16;
typedef unsigned v4u __attribute__((ext_vector_type(4)));
typedef unsigned v2u __attribute__((ext_vector_type(2)));
typedef float f4 __attribute__((ext_vector_type(4)));
typedef float f2 __attribute__((ext_vector_type(2)));
typedef __bf16 bf8 __attribute__((ext_vector_type(8)));
typedef __bf16 bf2 __attribute__((ext_vector_type(2)));
typedef short s4 __attribute__((ext_vector_type(4)));
typedef __amdgpu_buffer_rsrc_t rsrc_t;
#define LAS __attribute__((address_space(3)))

constexpr int NB = 4, D = 640, NH = 32, DH = 128, SK = 4096, WIN = 1024, FF = 1728, HD = 4096;
constexpr unsigned SENT = 0xFFFFFFFFu;
constexpr int OOB = 0x40000000;
constexpr int SZ_Q = 32 * 4 * 256, SZ_O = 32 * 4 * 256, SZ_X1 = 4 * 40 * 128, SZ_H = 108 * 128, SZ_X2 = 40 * 128;
constexpr int OFF_Q = 0, OFF_O = OFF_Q + 2 * SZ_Q, OFF_X1 = OFF_O + 2 * SZ_O, OFF_H = OFF_X1 + 2 * SZ_X1,
              OFF_X2 = OFF_H + 2 * SZ_H, OFF_FLAG = OFF_X2 + SZ_X2, OFF_EP = OFF_FLAG + 2048, WS_USED = OFF_EP + 128;
constexpr int OFF_STAMP = 1 << 20;
constexpr int SPIN_CAP = 1 << 17;
constexpr int L_V = 0;
constexpr int L_P = 131072;
constexpr int L_OW = L_P + 2048;
constexpr int L_ML = L_OW + 4096;
constexpr int L_Q = L_ML + 64;
constexpr int L_QF = L_Q + 256;
constexpr int L_ST = L_QF + 16;
constexpr int L_END = L_ST + (MK_STAMPS ? 1024 : 0);
constexpr int L_RED = 0;
constexpr int RED_W = 5 * 36 * 4;

constexpr int AUX_NT = 18, AUX_SC1 = 16;

__device__ __forceinline__ f4 mfma(v4u a, v4u b, f4 c) {
  return __builtin_amdgcn_mfma_f32_16x16x32_bf16(__builtin_bit_cast(bf8, a), __builtin_bit_cast(bf8, b), c, 0, 0, 0);
}
__device__ __forceinline__ unsigned hi16pack(float hi, float lo) {
  return __builtin_amdgcn_perm(__builtin_bit_cast(unsigned, hi), __builtin_bit_cast(unsigned, lo), 0x07060302u);
}
__device__ __forceinline__ unsigned pack_bf16(float lo, float hi) {
  bf2 r = {(__bf16)lo, (__bf16)hi};
  unsigned u = __builtin_bit_cast(unsigned, r);
  return u == SENT ? 0x7FFF7FFFu : u;
}
__device__ __forceinline__ float bflo(unsigned u) { return __builtin_bit_cast(float, u << 16); }
__device__ __forceinline__ float bfhi(unsigned u) { return __builtin_bit_cast(float, u & 0xFFFF0000u); }
__device__ __forceinline__ float dot2(unsigned a, unsigned b, float c) {
  return __builtin_amdgcn_fdot2_f32_bf16(__builtin_bit_cast(bf2, a), __builtin_bit_cast(bf2, b), c, false);
}
template <int CTRL> __device__ __forceinline__ float dpp(float x) {
  return __builtin_bit_cast(float, __builtin_amdgcn_mov_dpp(__builtin_bit_cast(int, x), CTRL, 0xf, 0xf, true));
}
constexpr int XOR1 = 0xB1, XOR2 = 0x4E, ROR4 = 0x124, ROR8 = 0x128, SHL4 = 0x104;
__device__ __forceinline__ float xrow_sum(float x) {
  auto s = __builtin_amdgcn_permlane16_swap(__float_as_uint(x), __float_as_uint(x), false, false);
  x = __uint_as_float(s[0]) + __uint_as_float(s[1]);
  auto t = __builtin_amdgcn_permlane32_swap(__float_as_uint(x), __float_as_uint(x), false, false);
  return __uint_as_float(t[0]) + __uint_as_float(t[1]);
}
__device__ __forceinline__ float xrow_max(float x) {
  auto s = __builtin_amdgcn_permlane16_swap(__float_as_uint(x), __float_as_uint(x), false, false);
  x = fmaxf(__uint_as_float(s[0]), __uint_as_float(s[1]));
  auto t = __builtin_amdgcn_permlane32_swap(__float_as_uint(x), __float_as_uint(x), false, false);
  return fmaxf(__uint_as_float(t[0]), __uint_as_float(t[1]));
}
__device__ __forceinline__ float x32_sum(float x) {
  auto t = __builtin_amdgcn_permlane32_swap(__float_as_uint(x), __float_as_uint(x), false, false);
  return __uint_as_float(t[0]) + __uint_as_float(t[1]);
}
__device__ __forceinline__ rsrc_t mkrsrc(const void* p, unsigned bytes) {
  return __builtin_amdgcn_make_buffer_rsrc((void*)p, 0, bytes, 0x00020000);
}
template <int AUX> __device__ __forceinline__ v4u ld128(rsrc_t rs, int voff, int soff) {
  return __builtin_amdgcn_raw_buffer_load_b128(rs, voff, soff, AUX);
}
__device__ __forceinline__ unsigned umax3(unsigned a, unsigned b, unsigned c) { return max(max(a, b), c); }
__device__ __forceinline__ unsigned vmax4(v4u x, unsigned m) { return umax3(x.x, x.y, umax3(x.z, x.w, m)); }

#define DLY 12
#define SETTLE() __builtin_amdgcn_s_sleep(DLY)
template <int N> __device__ __forceinline__ void gather(v4u (&x)[N], rsrc_t rs, const int (&off)[N], bool& dead) {
  for (int spin = 0;; ++spin) {
    asm volatile("" ::: "memory");
#pragma unroll
    for (int i = 0; i < N; ++i) x[i] = ld128<AUX_SC1>(rs, off[i], 0);
    unsigned m = 0u;
#pragma unroll
    for (int i = 0; i < N; ++i) m = vmax4(x[i], m);
    if (!__any(m == SENT)) break;
    if (dead || spin > SPIN_CAP) { dead = true; break; }
    __builtin_amdgcn_s_sleep(2);
  }
}
__device__ __forceinline__ unsigned long long epoch_load(rsrc_t rws) {
  const v2u e = __builtin_amdgcn_raw_buffer_load_b64(rws, OFF_EP, 0, AUX_SC1);
  return ((unsigned long long)(unsigned)__builtin_amdgcn_readfirstlane(e.y) << 32 | (unsigned)__builtin_amdgcn_readfirstlane(e.x)) + 1ull;
}
__device__ __forceinline__ void clear16(rsrc_t rws, int off) {
  __builtin_amdgcn_raw_buffer_store_b128(v4u{SENT, SENT, SENT, SENT}, rws, off, 0, AUX_SC1);
}
__device__ __forceinline__ void flag_post(rsrc_t rws, int cu, unsigned long long tag, int l) {
  asm volatile("s_waitcnt vmcnt(0)" ::: "memory");
  if (l == 0) __builtin_amdgcn_raw_buffer_store_b64(v2u{(unsigned)tag, (unsigned)(tag >> 32)}, rws, OFF_FLAG + cu * 8, 0, AUX_SC1);
}
__device__ __forceinline__ void flags_wait(rsrc_t rws, unsigned long long tag, int l, bool& dead) {
  const unsigned lo = (unsigned)tag, hi = (unsigned)(tag >> 32);
  for (int spin = 0;; ++spin) {
    asm volatile("" ::: "memory");
    const v4u a = ld128<AUX_SC1>(rws, OFF_FLAG + l * 32, 0), b = ld128<AUX_SC1>(rws, OFF_FLAG + l * 32 + 16, 0);
    const bool ok = (a.x == lo) & (a.y == hi) & (a.z == lo) & (a.w == hi) & (b.x == lo) & (b.y == hi) & (b.z == lo) & (b.w == hi);
    if (__all(ok)) break;
    if (dead || spin > SPIN_CAP) { dead = true; break; }
    __builtin_amdgcn_s_sleep(4);
  }
}
__device__ __forceinline__ v4u xpose16(v4u raw, v4u sel1, v4u sel2) {
  const f4 z = {0.f, 0.f, 0.f, 0.f};
  const f4 d1 = mfma(raw, sel1, z), d2 = mfma(raw, sel2, z);
  return v4u{hi16pack(d1[1], d1[0]), hi16pack(d1[3], d1[2]), hi16pack(d2[1], d2[0]), hi16pack(d2[3], d2[2])};
}
__device__ __forceinline__ void xpose32(v4u& a, v4u& b, v4u sel1, v4u sel2) {
  const f4 z = {0.f, 0.f, 0.f, 0.f};
  const f4 a1 = mfma(a, sel1, z), b1 = mfma(b, sel1, z), a2 = mfma(a, sel2, z), b2 = mfma(b, sel2, z);
  a = v4u{hi16pack(a1[1], a1[0]), hi16pack(a1[3], a1[2]), hi16pack(b1[1], b1[0]), hi16pack(b1[3], b1[2])};
  b = v4u{hi16pack(a2[1], a2[0]), hi16pack(a2[3], a2[2]), hi16pack(b2[1], b2[0]), hi16pack(b2[3], b2[2])};
}

#define STAMP(i) do { } while (0)
#define STAMP2(p, a, b) do { } while (0)

#define ASM_LD(dst, voff, rs, soff, MODS) \
  asm volatile("buffer_load_dwordx4 %0, %1, %2, %3 offen " MODS : "=v"(dst) : "v"(voff), "s"(rs), "s"(soff) : "memory")
#define ASM_DMA(voff, rs, soff, ldsa) \
  asm volatile("s_mov_b32 m0, %3\n\ts_nop 0\n\tbuffer_load_dwordx4 %0, %1, %2 offen sc1 nt lds" ::"v"(voff), "s"(rs), "s"(soff), "s"(ldsa) : "memory")
#define WAIT_VM(n) asm volatile("s_waitcnt vmcnt(" #n ")" ::: "memory")
#define PIN8(a, o) asm volatile("" : "+v"(a[o]), "+v"(a[o + 1]), "+v"(a[o + 2]), "+v"(a[o + 3]), "+v"(a[o + 4]), "+v"(a[o + 5]), "+v"(a[o + 6]), "+v"(a[o + 7]))

__device__ __forceinline__ v2u tr_read(LAS unsigned char* p) {
  s4 t = __builtin_amdgcn_ds_read_tr16_b64_v4i16((LAS s4*)p);
  return __builtin_bit_cast(v2u, t);
}

__device__ __forceinline__ void attn_math(const int PASS, LAS unsigned char* L, const v4u (&kf)[32], const v4u (&vf)[16], const v4u (&qf)[4],
                                          rsrc_t rws, int b, int h, int w, int l_, LAS unsigned* st) {
  int l = l_; asm volatile("" : "+v"(l));
  const int g = l >> 4;
  const f4 z = {0.f, 0.f, 0.f, 0.f};
  constexpr float CS = 0.08838834764831845f * 1.4426950408889634f;
  float s0 = 0.f, s1 = 0.f;
#pragma unroll
  for (int t = 0; t < 8; t += 2) {
    f4 d0 = z, d1 = z;
#pragma unroll
    for (int s = 0; s < 4; ++s) { d0 = mfma(qf[s], kf[t * 4 + s], d0); d1 = mfma(qf[s], kf[t * 4 + 4 + s], d1); }
    if (t < 4) { s0 = (g == t) ? d0[0] : s0; s0 = (g == t + 1) ? d1[0] : s0; }
    else { s1 = (g == t - 4) ? d0[0] : s1; s1 = (g == t - 3) ? d1[0] : s1; }
  }
  STAMP2(PASS, 16, 8);
  float m = fmaxf(s0, s1);
  m = fmaxf(m, dpp<XOR1>(m)); m = fmaxf(m, dpp<XOR2>(m)); m = fmaxf(m, dpp<ROR4>(m)); m = fmaxf(m, dpp<ROR8>(m));
  m = xrow_max(m);
  const __bf16 p0 = (__bf16)__builtin_amdgcn_exp2f((s0 - m) * CS), p1 = (__bf16)__builtin_amdgcn_exp2f((s1 - m) * CS);
  LAS __bf16* pw = (LAS __bf16*)(L + L_P + w * 256);
  pw[l] = p0; pw[64 + l] = p1;
  v4u pf[4];
#pragma unroll
  for (int s = 0; s < 4; ++s) pf[s] = *(LAS v4u*)(L + L_P + w * 256 + s * 64 + g * 16);
  STAMP2(PASS, 17, 9);
  float ls = (float)p0 + (float)p1;
  ls += dpp<XOR1>(ls); ls += dpp<XOR2>(ls); ls += dpp<ROR4>(ls); ls += dpp<ROR8>(ls);
  ls = xrow_sum(ls);
  float oa0 = 0.f, oa1 = 0.f;
#pragma unroll
  for (int n = 0; n < 8; n += 2) {
    f4 d0 = mfma(pf[0], vf[n], z), d1 = mfma(pf[0], vf[n + 1], z);
    d0 = mfma(pf[1], vf[8 + n], d0); d1 = mfma(pf[1], vf[9 + n], d1);
    if (n < 4) { oa0 = (g == n) ? d0[0] : oa0; oa0 = (g == n + 1) ? d1[0] : oa0; }
    else { oa1 = (g == n - 4) ? d0[0] : oa1; oa1 = (g == n - 3) ? d1[0] : oa1; }
  }
  WAIT_VM(0);
  STAMP2(PASS, 2, 6);
  const int q4 = (l >> 2) & 3;
  const int trb = L_V + w * 16384 + (8 * g + q4) * 256 + ((q4 | ((g & 1) << 2)) << 5) + (l & 3) * 8;
  float ob0 = 0.f, ob1 = 0.f;
  v2u t0[2], t1[2], t2[2], t3[2];
#define TR_LOAD(n) do { LAS unsigned char* a_ = L + (trb ^ ((n) << 5)); \
    t0[(n) & 1] = tr_read(a_); t1[(n) & 1] = tr_read(a_ + 1024); t2[(n) & 1] = tr_read(a_ + 8192); t3[(n) & 1] = tr_read(a_ + 8192 + 1024); } while (0)
  TR_LOAD(0);
#pragma unroll
  for (int n = 0; n < 8; ++n) {
    if (n < 7) TR_LOAD(n + 1);
    __builtin_amdgcn_sched_barrier(0);
    f4 d = mfma(pf[2], v4u{t0[n & 1].x, t0[n & 1].y, t1[n & 1].x, t1[n & 1].y}, z);
    d = mfma(pf[3], v4u{t2[n & 1].x, t2[n & 1].y, t3[n & 1].x, t3[n & 1].y}, d);
    if (n < 4) ob0 = (g == n) ? d[0] : ob0; else ob1 = (g == n - 4) ? d[0] : ob1;
  }
  LAS float* ow = (LAS float*)(L + L_OW);
  ow[w * 128 + l] = oa0 + ob0; ow[w * 128 + 64 + l] = oa1 + ob1;
  if (l == 0) *(LAS f2*)(L + L_ML + w * 8) = f2{m, ls};
  STAMP2(PASS, 18, 10);
  __syncthreads();
  STAMP2(PASS, 19, 11);
  if (w == 0) {
    f4 ml[4];
    f2 ov[8];
#pragma unroll
    for (int i = 0; i < 4; ++i) ml[i] = *(LAS f4*)(L + L_ML + i * 16);
#pragma unroll
    for (int i = 0; i < 8; ++i) ov[i] = *(LAS f2*)(L + L_OW + i * 512 + l * 8);
    float M = fmaxf(fmaxf(fmaxf(ml[0][0], ml[0][2]), fmaxf(ml[1][0], ml[1][2])), fmaxf(fmaxf(ml[2][0], ml[2][2]), fmaxf(ml[3][0], ml[3][2])));
    float Ls = 0.f, o0 = 0.f, o1 = 0.f;
#pragma unroll
    for (int i = 0; i < 8; ++i) {
      const float f = __builtin_amdgcn_exp2f((ml[i >> 1][(i & 1) * 2] - M) * CS);
      Ls = fmaf(f, ml[i >> 1][(i & 1) * 2 + 1], Ls); o0 = fmaf(f, ov[i].x, o0); o1 = fmaf(f, ov[i].y, o1);
    }
    const float inv = __builtin_amdgcn_rcpf(Ls);
    __builtin_amdgcn_raw_buffer_store_b32(pack_bf16(o0 * inv, o1 * inv), rws, OFF_O + PASS * SZ_O + (h * 4 + b) * 256 + l * 4, 0, AUX_SC1);
  }
  STAMP2(PASS, 3, 7);
}

__device__ __forceinline__ void attn_role(LAS unsigned char* L, const u16* K, const u16* V, rsrc_t rws, int cu, int xc, int y, int w, int l,
                                          LAS unsigned* st) {
  const int h = 4 * xc + (y >> 2), b = y & 3, g = l >> 4, r = l & 15;
  const size_t kv = ((size_t)(b * NH + h) * SK + (SK - WIN) + 128 * w) * DH;
  const rsrc_t rK = mkrsrc(K + kv, 32768), rV = mkrsrc(V + kv, 32768);
  constexpr int PSTR = 8192;
  bool dead = false;
  if (threadIdx.x == 0) *(volatile LAS unsigned*)(L + L_QF) = 0u;
  asm volatile("s_waitcnt lgkmcnt(0)\n\ts_barrier" ::: "memory");
  v2u ep = {0u, 0u};
  unsigned long long tag = 0;
  asm volatile("buffer_load_dwordx2 %0, %1, %2, 0 offen sc1" : "=v"(ep) : "v"(w == 0 ? OFF_EP : OOB), "s"(rws) : "memory");
  {
    const v4u sent = {SENT, SENT, SENT, SENT};
    const int co = (w == 0 && l < 32) ? OFF_O + (l >> 4) * SZ_O + (h * 4 + b) * 256 + r * 16 : OOB;
    asm volatile("buffer_store_dwordx4 %0, %1, %2, 0 offen sc1\n\ts_nop 1" ::"v"(sent), "v"(co), "s"(rws) : "memory");
  }
#define SVC_AT 3
#define SVC(n) do { if (SVC_AT == (n) && w == 0) { \
      flags_wait(rws, tag, l, dead); \
      v4u q1[1]; const int off[1] = {l < 16 ? OFF_Q + (h * 16 + b) * 64 + (l >> 2) * 256 + (l & 3) * 16 : OOB}; \
      gather<1>(q1, rws, off, dead); \
      if (l < 16) *(LAS v4u*)(L + L_Q + l * 16) = q1[0]; \
      asm volatile("s_waitcnt lgkmcnt(0)" ::: "memory"); \
      if (l == 0) *(volatile LAS unsigned*)(L + L_QF) = 1u; } } while (0)
  v4u kf[32], vf[16], qf[4];
  const int ldsV = L_V + w * 16384;
  const int ks0 = g * 256 + ((r ^ g) << 4);
#define KX(i) (((i) & 3) << 6)
  const int vs0 = g * 256 + (((r >> 1) ^ g) << 5) + (l & 1) * 16, vs1 = g * 256 + (((r >> 1) ^ (g | 4)) << 5) + (l & 1) * 16;
#define K_PIECE(n) _Pragma("unroll") for (int i = 0; i < 8; ++i) \
    ASM_DMA(ks0 ^ KX(i), rK, (n) * PSTR + i * 1024, ldsV + ((n) & 1) * 8192 + i * 1024)
#define V_PIECE(n, buf) _Pragma("unroll") for (int i = 0; i < 8; ++i) \
    ASM_DMA((i & 2) ? vs1 : vs0, rV, (n) * PSTR + i * 1024, ldsV + (buf) * 8192 + i * 1024)
#define K_READ(n) do { _Pragma("unroll") for (int t = 0; t < 2; ++t) _Pragma("unroll") for (int s = 0; s < 4; ++s) \
      kf[((n) * 2 + t) * 4 + s] = *(LAS v4u*)(L + ldsV + ((n) & 1) * 8192 + (16 * t + r) * 256 + (((4 * s + g) ^ r) << 4)); \
    asm volatile("s_waitcnt lgkmcnt(0)" ::: "memory"); PIN8(kf, (n) * 8); } while (0)
  const int trb = ldsV + (8 * g + ((l >> 2) & 3)) * 256 + ((((l >> 2) & 3) | ((g & 1) << 2)) << 5) + (l & 3) * 8;
#define V_READ(n) do { _Pragma("unroll") for (int c8 = 0; c8 < 8; ++c8) { \
      LAS unsigned char* a = L + (trb ^ (c8 << 5)) + (n) * 8192; \
      const v2u t0 = tr_read(a), t1 = tr_read(a + 1024); \
      vf[(n) * 8 + c8] = v4u{t0.x, t0.y, t1.x, t1.y}; } \
    asm volatile("s_waitcnt lgkmcnt(0)" ::: "memory"); PIN8(vf, (n) * 8); } while (0)
  K_PIECE(0); K_PIECE(1);
  asm volatile("s_waitcnt vmcnt(16)" : "+v"(ep)::"memory");
  tag = ((unsigned long long)(unsigned)__builtin_amdgcn_readfirstlane(ep.y) << 32 | (unsigned)__builtin_amdgcn_readfirstlane(ep.x)) + 1ull;
  {
    const v2u tg = {(unsigned)tag, (unsigned)(tag >> 32)};
    asm volatile("buffer_store_dwordx2 %0, %1, %2, 0 offen sc1" ::"v"(tg), "v"((w == 0 && l == 0) ? OFF_FLAG + cu * 8 : OOB), "s"(rws) : "memory");
  }
  WAIT_VM(9);
  K_READ(0); K_PIECE(2);
  WAIT_VM(8); K_READ(1); K_PIECE(3); SVC(1);
  WAIT_VM(8); K_READ(2); V_PIECE(0, 0); SVC(2);
  WAIT_VM(8); K_READ(3); V_PIECE(1, 1); SVC(3);
  WAIT_VM(8); V_READ(0); V_PIECE(2, 0); SVC(4);
  WAIT_VM(8); V_READ(1); V_PIECE(3, 1); SVC(5);
  STAMP(1);
#pragma unroll 1
  for (int pass = 0; pass < 2; ++pass) {
    if (pass == 0) {
      for (int spin = 0; *(volatile LAS unsigned*)(L + L_QF) == 0u; ++spin) {
        if (spin > (SPIN_CAP << 4)) { dead = true; break; }
        __builtin_amdgcn_s_sleep(1);
      }
#pragma unroll
      for (int s = 0; s < 4; ++s) qf[s] = *(LAS v4u*)(L + L_Q + s * 64 + g * 16);
      asm volatile("" ::: "memory");
    } else {
      int lo = l; asm volatile("" : "+v"(lo));
      const int qo = OFF_Q + SZ_Q + (h * 16 + b) * 64 + (lo >> 4) * 16;
      const int off[4] = {qo, qo + 256, qo + 512, qo + 768};
      gather<4>(qf, rws, off, dead);
    }
    STAMP2(pass, 4, 5);
    attn_math(pass, L, kf, vf, qf, rws, b, h, w, l, st);
  }
}

__device__ __forceinline__ f4 red_sum(LAS unsigned char* L, int row, int l) {
  const int hf = l >> 5, cg = l & 7;
  f4 s = *(LAS f4*)(L + L_RED + (hf * 4) * RED_W + row * 144 + cg * 16);
#pragma unroll
  for (int i = 1; i < 4; ++i) s += *(LAS f4*)(L + L_RED + (hf * 4 + i) * RED_W + row * 144 + cg * 16);
  return f4{x32_sum(s[0]), x32_sum(s[1]), x32_sum(s[2]), x32_sum(s[3])};
}
__device__ __forceinline__ f2 red_stat(LAS unsigned char* L, int row, int l) {
  const int hf = l >> 5;
  f2 s = *(LAS f2*)(L + L_RED + (hf * 4) * RED_W + row * 144 + 128);
#pragma unroll
  for (int i = 1; i < 4; ++i) s += *(LAS f2*)(L + L_RED + (hf * 4 + i) * RED_W + row * 144 + 128);
  return f2{x32_sum(s[0]), x32_sum(s[1])};
}

__device__ __forceinline__ f4 red_sum_gu(LAS unsigned char* L, int row, int l) {
  const int m = l >> 5, cg = l & 7;
  f4 s = *(LAS f4*)(L + L_RED + m * RED_W + row * 144 + cg * 16);
#pragma unroll
  for (int i = 1; i < 4; ++i) s += *(LAS f4*)(L + L_RED + (2 * i + m) * RED_W + row * 144 + cg * 16);
  return s;
}
struct WArgs { const u16 *x, *wq, *wo, *wg, *wu, *wd; u16* out; };

template <int PASS>
__device__ __forceinline__ void q_phase(LAS unsigned char* L, const v4u (&xf)[3], const v4u (&wqf)[6], f4 csq, rsrc_t rws, int tq, int w, int l, LAS unsigned* st) {
  const int g = l >> 4, c = l & 15;
  const f4 z = {0.f, 0.f, 0.f, 0.f};
  const v4u ones = {0x3F803F80u, 0x3F803F80u, 0x3F803F80u, 0x3F803F80u};
  f4 d0 = z, d1 = z, ds = z;
  float ss = 0.f;
#pragma unroll
  for (int i = 0; i < 3; ++i) {
    d0 = mfma(wqf[2 * i], xf[i], d0); d1 = mfma(wqf[2 * i + 1], xf[i], d1); ds = mfma(ones, xf[i], ds);
    ss = dot2(xf[i].x, xf[i].x, ss); ss = dot2(xf[i].y, xf[i].y, ss); ss = dot2(xf[i].z, xf[i].z, ss); ss = dot2(xf[i].w, xf[i].w, ss);
  }
  ss = xrow_sum(ss);
  LAS unsigned char* R = L + L_RED + w * RED_W + c * 144;
  if (c < 4) {
    *(LAS f4*)(R + g * 16) = d0; *(LAS f4*)(R + 64 + g * 16) = d1;
    if (g == 0) *(LAS f2*)(R + 128) = f2{ds[0], ss};
  }
  __syncthreads();
  if (PASS == 1) STAMP(22);
  if (w == 0) {
    const int b = (l >> 3) & 3, cg = l & 7;
    const f4 p = red_sum(L, b, l);
    const f2 s = red_stat(L, b, l);
    const float mu = s[0] * (1.0f / D), var = fmaxf(s[1] * (1.0f / D) - mu * mu, 0.f), rs = __builtin_amdgcn_rsqf(var + 1e-5f);
    if (l < 32) {
      const v2u o = {pack_bf16((p[0] - mu * csq[0]) * rs, (p[1] - mu * csq[1]) * rs), pack_bf16((p[2] - mu * csq[2]) * rs, (p[3] - mu * csq[3]) * rs)};
      __builtin_amdgcn_raw_buffer_store_b64(o, rws, OFF_Q + PASS * SZ_Q + tq * 256 + b * 64 + cg * 8, 0, AUX_SC1);
    }
  }
  __syncthreads();
}

__device__ __forceinline__ void q_second(LAS unsigned char* L, const v4u (&wqf)[6], f4 csq, rsrc_t rws, int tq, int w, int l, bool& dead,
                                         LAS unsigned* st) {
  const int g = l >> 4, c = l & 15;
  v4u xf[3];
  int off[3];
#pragma unroll
  for (int i = 0; i < 3; ++i) {
    const int s = w + 8 * i;
    off[i] = (c < 4 && s < 20) ? OFF_X2 + ((2 * s + (g >> 1)) * 4 + c) * 32 + (g & 1) * 16 : OOB;
  }
  gather<3>(xf, rws, off, dead);
  STAMP(6);
  q_phase<1>(L, xf, wqf, csq, rws, tq, w, l, st);
  STAMP(7);
}

__device__ __forceinline__ void weight_role(LAS unsigned char* L, const WArgs& a, rsrc_t rws, int cu, int xc, int u, int w, int l,
                                            LAS unsigned* st) {
  const int g = l >> 4, r = l & 15, c = r;
  const f4 z = {0.f, 0.f, 0.f, 0.f};
  const v4u ones = {0x3F803F80u, 0x3F803F80u, 0x3F803F80u, 0x3F803F80u};
  v4u sel1, sel2;
  {
    const int j = r - 8 * (g & 1);
    const unsigned one = (j & 1) ? 0x3F800000u : 0x00003F80u;
    const bool in = (unsigned)j < 8u;
    v4u s = {(in && (j >> 1) == 0) ? one : 0u, (in && (j >> 1) == 1) ? one : 0u, (in && (j >> 1) == 2) ? one : 0u, (in && (j >> 1) == 3) ? one : 0u};
    const v4u zz = {0u, 0u, 0u, 0u};
    sel1 = g < 2 ? s : zz; sel2 = g < 2 ? zz : s;
  }
  const int tq = xc * 16 + u;
  const bool has_wo = u < 10, has_gu = u < (xc < 3 ? 8 : 6), has_dn = u >= 10 && u < 15;
  const int wo_id = xc * 10 + u, kq = wo_id / 20, nt32 = wo_id % 20;
  const int t32 = xc < 3 ? xc * 8 + u : 24 + (xc - 3) * 6 + u;
  const int td = xc * 5 + (u - 10);
  const int rho16 = 8 * (r >> 2) + (r & 3);
  const int rho32 = rho16 + 4 * (g >> 1);

  bool dead = false;
  const unsigned long long tstart = __builtin_amdgcn_s_memrealtime();
  v2u ep = {0u, 0u};
  if (w == 0) {
    ep = __builtin_amdgcn_raw_buffer_load_b64(rws, OFF_EP, 0, AUX_SC1);
    const int hf = l >> 5, k = l & 31;
    clear16(rws, k < 16 ? OFF_Q + hf * SZ_Q + tq * 256 + k * 16 : has_wo ? OFF_X1 + hf * SZ_X1 + (kq * 40 + 2 * nt32) * 128 + (k - 16) * 16 : OOB);
    clear16(rws, (k < 16 && has_gu) ? OFF_H + hf * SZ_H + t32 * 256 + k * 16 : (k < 8 && hf == 0 && has_dn) ? OFF_X2 + td * 128 + k * 16 : OOB);
  }
  v4u wqf[6];
  unsigned long long tag = 0;
  f4 csq;
  {
    v4u xf[3];
    const rsrc_t rx = mkrsrc(a.x, NB * D * 2), rq = mkrsrc(a.wq, D * HD * 2);
#pragma unroll
    for (int i = 0; i < 3; ++i) {
      const int s = w + 8 * i;
      xf[i] = ld128<0>(rx, (c < 4 && s < 20) ? c * 1280 + s * 64 + g * 16 : OOB, 0);
    }
#pragma unroll
    for (int i = 0; i < 3; ++i) {
      const int s = w + 8 * i;
      const int vo = s < 20 ? (32 * s + rho16) * 8192 + tq * 64 + g * 16 : OOB;
      wqf[2 * i] = ld128<AUX_NT>(rq, vo, 0); wqf[2 * i + 1] = ld128<AUX_NT>(rq, vo, 4 * 8192);
    }
    __builtin_amdgcn_sched_barrier(0);
    if (w == 0) {
      asm volatile("s_waitcnt vmcnt(9)" : "+v"(ep)::"memory");
      tag = ((unsigned long long)(unsigned)__builtin_amdgcn_readfirstlane(ep.y) << 32 | (unsigned)__builtin_amdgcn_readfirstlane(ep.x)) + 1ull;
      if (l == 0) __builtin_amdgcn_raw_buffer_store_b64(v2u{(unsigned)tag, (unsigned)(tag >> 32)}, rws, OFF_FLAG + cu * 8, 0, AUX_SC1);
    }
#pragma unroll
    for (int i = 0; i < 3; ++i) xpose32(wqf[2 * i], wqf[2 * i + 1], sel1, sel2);
    f4 c0 = z, c1 = z;
#pragma unroll
    for (int i = 0; i < 3; ++i) { c0 = mfma(wqf[2 * i], ones, c0); c1 = mfma(wqf[2 * i + 1], ones, c1); }
    LAS unsigned char* R4 = L + L_RED + w * RED_W + 4 * 144;
    if (c == 0) { *(LAS f4*)(R4 + g * 16) = c0; *(LAS f4*)(R4 + 64 + g * 16) = c1; }
    __syncthreads();
    csq = red_sum(L, 4, l);
    q_phase<0>(L, xf, wqf, csq, rws, tq, w, l, st);
  }
  STAMP(1);
  const int fb = (l >> 3) & 3, fcg = l & 7;
  LAS unsigned char* R = L + L_RED + w * RED_W + c * 144;
#define HOLD() do { while ((long long)(__builtin_amdgcn_s_memrealtime() - tstart) < HOLD_T) __builtin_amdgcn_s_sleep(16); } while (0)

  if (has_wo) {
    v4u wof[8], guf[10];
    const int gm = w & 1, gq = w >> 1;
    {
      const rsrc_t ro = mkrsrc(a.wo, D * HD * 2), rgu = mkrsrc(gm ? a.wu : a.wg, D * FF * 2);
      const int vo = ((8 * kq + w) * 128 + rho16) * 1280 + nt32 * 64 + g * 16;
#pragma unroll
      for (int s = 0; s < 4; ++s) { wof[2 * s] = ld128<AUX_NT>(ro, vo, s * 32 * 1280); wof[2 * s + 1] = ld128<AUX_NT>(ro, vo, (s * 32 + 4) * 1280); }
      const int vg = has_gu ? (160 * gq + rho16) * 3456 + t32 * 64 + g * 16 : OOB;
#pragma unroll
      for (int i = 0; i < 5; ++i) { guf[2 * i] = ld128<AUX_NT>(rgu, vg, i * 32 * 3456); guf[2 * i + 1] = ld128<AUX_NT>(rgu, vg, (i * 32 + 4) * 3456); }
      __builtin_amdgcn_sched_barrier(0);
    }
#pragma unroll
    for (int s = 0; s < 4; ++s) xpose32(wof[2 * s], wof[2 * s + 1], sel1, sel2);
#pragma unroll
    for (int i = 0; i < 5; ++i) xpose32(guf[2 * i], guf[2 * i + 1], sel1, sel2);
    f4 csg;
    {
      f4 c0 = z, c1 = z;
#pragma unroll
      for (int i = 0; i < 5; ++i) { c0 = mfma(guf[2 * i], ones, c0); c1 = mfma(guf[2 * i + 1], ones, c1); }
      LAS unsigned char* R4 = L + L_RED + w * RED_W + 4 * 144;
      if (c == 0) { *(LAS f4*)(R4 + g * 16) = c0; *(LAS f4*)(R4 + 64 + g * 16) = c1; }
      if (w == 0) flags_wait(rws, tag, l, dead);
      __syncthreads();
      csg = red_sum_gu(L, 4, l);
    }
    STAMP(2);
    HOLD();
#pragma unroll 1
    for (int pass = 0; pass < 2; ++pass) {
      {
        v4u of[4];
        int lo = l; asm volatile("" : "+v"(lo));
        const int o0 = (lo & 15) < 4 ? OFF_O + pass * SZ_O + ((8 * kq + w) * 4 + (lo & 15)) * 256 + (lo >> 4) * 16 : OOB;
        const int off[4] = {o0, o0 + 64, o0 + 128, o0 + 192};
        v2u res = {0u, 0u};
        if (w == 0 && kq == 0 && l < 32) {
          if (pass == 0) res = *(const v2u*)(a.x + fb * D + nt32 * 32 + fcg * 4);
          else res = __builtin_amdgcn_raw_buffer_load_b64(rws, OFF_X2 + ((2 * nt32 + (fcg >> 2)) * 4 + fb) * 32 + (fcg & 3) * 8, 0, AUX_SC1);
        }
        gather<4>(of, rws, off, dead);
        STAMP2(pass, 3, 9);
        f4 d0 = z, d1 = z;
#pragma unroll
        for (int s = 0; s < 4; ++s) { d0 = mfma(wof[2 * s], of[s], d0); d1 = mfma(wof[2 * s + 1], of[s], d1); }
        if (c < 4) { *(LAS f4*)(R + g * 16) = d0; *(LAS f4*)(R + 64 + g * 16) = d1; }
        __syncthreads();
        STAMP2(pass, 16, 24);
        if (w == 0) {
          f4 p = red_sum(L, fb, l);
          p[0] += bflo(res.x); p[1] += bfhi(res.x); p[2] += bflo(res.y); p[3] += bfhi(res.y);
          if (l < 32) {
            const v2u o = {pack_bf16(p[0], p[1]), pack_bf16(p[2], p[3])};
            __builtin_amdgcn_raw_buffer_store_b64(o, rws, OFF_X1 + pass * SZ_X1 + ((kq * 40 + 2 * nt32 + (fcg >> 2)) * 4 + fb) * 32 + (fcg & 3) * 8, 0, AUX_SC1);
          }
          STAMP2(pass, 17, 25);
        }
        __syncthreads();
      }
      if (has_gu) {
        v4u x1[5];
        int xo[5];
        int lo = l; asm volatile("" : "+v"(lo));
#pragma unroll
        for (int i = 0; i < 5; ++i)
          xo[i] = OFF_X1 + pass * SZ_X1 + ((((lo & 15) >> 2) * 40 + 2 * (5 * gq + i) + (lo >> 5)) * 4 + (lo & 3)) * 32 + ((lo >> 4) & 1) * 16;
        SETTLE();
        gather<5>(x1, rws, xo, dead);
        STAMP2(pass, 4, 10);
        f4 d0 = z, d1 = z, ds = z;
#pragma unroll
        for (int i = 0; i < 5; ++i) { d0 = mfma(guf[2 * i], x1[i], d0); d1 = mfma(guf[2 * i + 1], x1[i], d1); }
        if (gm == 0) {
#pragma unroll
          for (int i = 0; i < 5; ++i) ds = mfma(ones, x1[i], ds);
        }
#pragma unroll
        for (int i = 0; i < 4; ++i) {
          d0[i] += dpp<ROR4>(d0[i]); d0[i] += dpp<ROR8>(d0[i]);
          d1[i] += dpp<ROR4>(d1[i]); d1[i] += dpp<ROR8>(d1[i]);
        }
        float sx = ds[0]; sx += dpp<ROR4>(sx); sx += dpp<ROR8>(sx);
        if (c < 4) {
          *(LAS f4*)(R + g * 16) = d0; *(LAS f4*)(R + 64 + g * 16) = d1;
          if (g == 0) *(LAS float*)(R + 128) = sx;
        }
        __syncthreads();
        STAMP2(pass, 18, 26);
        if (w == 0) {
          const f4 p = red_sum_gu(L, fb, l);
          float sm = 0.f;
#pragma unroll
          for (int i = 0; i < 4; ++i) sm += *(LAS float*)(L + L_RED + 2 * i * RED_W + fb * 144 + 128);
          const float mu = sm * (1.0f / D);
          float hv[4];
#pragma unroll
          for (int i = 0; i < 4; ++i) {
            const float v = p[i] - mu * csg[i];
            auto sw = __builtin_amdgcn_permlane32_swap(__float_as_uint(v), __float_as_uint(v), false, false);
            hv[i] = fmaxf(__uint_as_float(sw[0]), 0.f) * __uint_as_float(sw[1]);
          }
          if (l < 32) {
            const v2u o = {pack_bf16(hv[0], hv[1]), pack_bf16(hv[2], hv[3])};
            __builtin_amdgcn_raw_buffer_store_b64(o, rws, OFF_H + pass * SZ_H + ((2 * t32 + (fcg >> 2)) * 4 + fb) * 32 + (fcg & 3) * 8, 0, AUX_SC1);
          }
          STAMP2(pass, 19, 27);
        }
        __syncthreads();
      }
      if (pass == 0) q_second(L, wqf, csq, rws, tq, w, l, dead, st);
    }
  } else {
    v4u wdf[7];
    {
      const rsrc_t rd = mkrsrc(a.wd, D * FF * 2);
#pragma unroll
      for (int i = 0; i < 7; ++i) {
        const int s = w + 8 * i;
        wdf[i] = ld128<AUX_NT>(rd, (has_dn && s < 54) ? (32 * s + rho32) * 1280 + td * 32 + (g & 1) * 16 : OOB, 0);
      }
      __builtin_amdgcn_sched_barrier(0);
    }
#pragma unroll
    for (int i = 0; i < 7; ++i) wdf[i] = xpose16(wdf[i], sel1, sel2);
    if (w == 0) flags_wait(rws, tag, l, dead);
    __syncthreads();
    STAMP(2);
    HOLD();
#pragma unroll 1
    for (int pass = 0; pass < 2; ++pass) {
      if (has_dn) {
        v4u x1[3];
        int xoff[3];
        int lo = l; asm volatile("" : "+v"(lo));
#pragma unroll
        for (int i = 0; i < 3; ++i) {
          const int s = w + 8 * i;
          xoff[i] = s < 20 ? OFF_X1 + pass * SZ_X1 + ((((lo & 15) >> 2) * 40 + 2 * s + (lo >> 5)) * 4 + (lo & 3)) * 32 + ((lo >> 4) & 1) * 16 : OOB;
        }
        gather<3>(x1, rws, xoff, dead);
        STAMP2(pass, 4, 10);
        f4 gram = z, ds = z;
#pragma unroll
        for (int i = 0; i < 3; ++i) { gram = mfma(x1[i], x1[i], gram); ds = mfma(ones, x1[i], ds); }
        float sq = (c & 3) == 0 ? gram[0] : (c & 3) == 1 ? gram[1] : (c & 3) == 2 ? gram[2] : gram[3];
        sq += dpp<ROR4>(sq); sq += dpp<ROR8>(sq); sq = xrow_sum(sq);
        float sx = ds[0]; sx += dpp<ROR4>(sx); sx += dpp<ROR8>(sx);
        if (c < 4 && g == 0) *(LAS f2*)(R + 128) = f2{sx, sq};
        __syncthreads();
        float rs2 = 0.f; f4 res = z;
        if (w == 0) {
          v2u rp[4];
#pragma unroll
          for (int k = 0; k < 4; ++k)
            rp[k] = __builtin_amdgcn_raw_buffer_load_b64(rws, OFF_X1 + pass * SZ_X1 + ((k * 40 + td) * 4 + fb) * 32 + (fcg & 3) * 8, 0, AUX_SC1);
          const f2 s = red_stat(L, fb, l);
          const float mu = s[0] * (1.0f / D), var = fmaxf(s[1] * (1.0f / D) - mu * mu, 0.f);
          rs2 = __builtin_amdgcn_rcpf(var + 1e-5f);
#pragma unroll
          for (int k = 0; k < 4; ++k) { res[0] += bflo(rp[k].x); res[1] += bfhi(rp[k].x); res[2] += bflo(rp[k].y); res[3] += bfhi(rp[k].y); }
        }
        v4u hf[7];
        int hoff[7];
#pragma unroll
        for (int i = 0; i < 7; ++i) {
          const int s = w + 8 * i;
          hoff[i] = ((lo & 15) < 4 && s < 54) ? OFF_H + pass * SZ_H + ((2 * s + (lo >> 5)) * 4 + (lo & 15)) * 32 + ((lo >> 4) & 1) * 16 : OOB;
        }
        gather<7>(hf, rws, hoff, dead);
        STAMP2(pass, 5, 11);
        f4 d = z;
#pragma unroll
        for (int i = 0; i < 7; ++i) d = mfma(wdf[i], hf[i], d);
        if (c < 4) { *(LAS f4*)(R + g * 16) = d; *(LAS f4*)(R + 64 + g * 16) = z; }
        __syncthreads();
        STAMP2(pass, 20, 28);
        if (w == 0) {
          const f4 p = red_sum(L, fb, l);
          if (l < 32 && fcg < 4) {
            f4 o;
#pragma unroll
            for (int i = 0; i < 4; ++i) o[i] = res[i] + rs2 * p[i];
            if (dead) o = f4{__builtin_nanf(""), __builtin_nanf(""), __builtin_nanf(""), __builtin_nanf("")};
            const v2u ob = {pack_bf16(o[0], o[1]), pack_bf16(o[2], o[3])};
            if (pass == 0) __builtin_amdgcn_raw_buffer_store_b64(ob, rws, OFF_X2 + (td * 4 + fb) * 32 + fcg * 8, 0, AUX_SC1);
            else *(v2u*)(a.out + fb * D + td * 16 + fcg * 4) = ob;
          }
          STAMP2(pass, 21, 29);
          if (pass == 1 && td == 0 && l == 0) __builtin_amdgcn_raw_buffer_store_b64(v2u{(unsigned)tag, (unsigned)(tag >> 32)}, rws, OFF_EP, 0, AUX_SC1);
        }
        __syncthreads();
      }
      if (pass == 0) { if (has_dn) SETTLE(); q_second(L, wqf, csq, rws, tq, w, l, dead, st); }
    }
  }
}

__global__ __launch_bounds__(512, 2) void mega_kernel(const u16* __restrict__ x, const u16* __restrict__ K, const u16* __restrict__ V,
                                                      const u16* __restrict__ wq, const u16* __restrict__ wo, const u16* __restrict__ wg,
                                                      const u16* __restrict__ wu, const u16* __restrict__ wd, u16* __restrict__ out,
                                                      unsigned char* __restrict__ ws) {
  __shared__ __attribute__((aligned(16))) unsigned char lds[L_END];
  LAS unsigned char* L = (LAS unsigned char*)lds;
  const int cu = blockIdx.x, xc = cu & 7, y = cu >> 3;
  const int l = threadIdx.x & 63, w = __builtin_amdgcn_readfirstlane(threadIdx.x >> 6);
  const rsrc_t rws = mkrsrc(ws, WS_USED);
  LAS unsigned* st = (LAS unsigned*)(L + L_ST) + w * 32;
  if (y < 16) attn_role(L, K, V, rws, cu, xc, y, w, l, st);
  else {
    WArgs a{x, wq, wo, wg, wu, wd, out};
    weight_role(L, a, rws, cu, xc, y - 16, w, l, st);
  }
}
}

extern "C" void kernel_launch(void* const* d_in, const int* in_sizes, int n_in, void* d_out, int out_size, void* d_ws, size_t ws_size,
                              hipStream_t stream) {
  (void)in_sizes; (void)n_in; (void)out_size; (void)ws_size;
  mega_kernel<<<256, 512, 0, stream>>>((const u16*)d_in[0], (const u16*)d_in[1], (const u16*)d_in[2], (const u16*)d_in[3],
                                       (const u16*)d_in[4], (const u16*)d_in[6], (const u16*)d_in[7], (const u16*)d_in[8],
                                       (u16*)d_out, (unsigned char*)d_ws);
}
```

```cpp
#include <hip/hip_runtime.h>
#include <stdint.h>

#define MK_STAMPS 0
#define HOLD_T 1100

namespace {
typedef unsigned short u16;
typedef unsigned v4u __attribute__((ext_vector_type(4)));
typedef unsigned v2u __attribute__((ext_vector_type(2)));
typedef float f4 __attribute__((ext_vector_type(4)));
typedef float f2 __attribute__((ext_vector_type(2)));
typedef __bf16 bf8 __attribute__((ext_vector_type(8)));
typedef __bf16 bf2 __attribute__((ext_vector_type(2)));
typedef short s4 __attribute__((ext_vector_type(4)));
typedef __amdgpu_buffer_rsrc_t rsrc_t;
#define LAS __attribute__((address_space(3)))

constexpr int NB = 4, D = 640, NH = 32, DH = 128, SK = 4096, WIN = 1024, FF = 1728, HD = 4096;
constexpr unsigned SENT = 0xFFFFFFFFu;
constexpr int OOB = 0x40000000;
constexpr int SZ_Q = 32 * 4 * 256, SZ_O = 32 * 4 * 256, SZ_X1 = 4 * 40 * 128, SZ_H = 108 * 128, SZ_X2 = 40 * 128;
constexpr int OFF_Q = 0, OFF_O = OFF_Q + 2 * SZ_Q, OFF_X1 = OFF_O + 2 * SZ_O, OFF_H = OFF_X1 + 2 * SZ_X1,
              OFF_X2 = OFF_H + 2 * SZ_H, OFF_FLAG = OFF_X2 + SZ_X2, OFF_EP = OFF_FLAG + 2048, WS_USED = OFF_EP + 128;
constexpr int OFF_STAMP = 1 << 20;
constexpr int SPIN_CAP = 1 << 17;
constexpr int L_V = 0;
constexpr int L_P = 131072;
constexpr int L_OW = L_P + 2048;
constexpr int L_ML = L_OW + 4096;
constexpr int L_Q = L_ML + 64;
constexpr int L_QF = L_Q + 256;
constexpr int L_ST = L_QF + 16;
constexpr int L_END = L_ST + (MK_STAMPS ? 1024 : 0);
constexpr int L_RED = 0;
constexpr int RED_W = 5 * 36 * 4;

constexpr int AUX_NT = 2, AUX_SC1 = 16;

__device__ __forceinline__ f4 mfma(v4u a, v4u b, f4 c) {
  return __builtin_amdgcn_mfma_f32_16x16x32_bf16(__builtin_bit_cast(bf8, a), __builtin_bit_cast(bf8, b), c, 0, 0, 0);
}
__device__ __forceinline__ unsigned hi16pack(float hi, float lo) {
  return __builtin_amdgcn_perm(__builtin_bit_cast(unsigned, hi), __builtin_bit_cast(unsigned, lo), 0x07060302u);
}
__device__ __forceinline__ unsigned pack_bf16(float lo, float hi) {
  bf2 r = {(__bf16)lo, (__bf16)hi};
  unsigned u = __builtin_bit_cast(unsigned, r);
  return u == SENT ? 0x7FFF7FFFu : u;
}
__device__ __forceinline__ float bflo(unsigned u) { return __builtin_bit_cast(float, u << 16); }
__device__ __forceinline__ float bfhi(unsigned u) { return __builtin_bit_cast(float, u & 0xFFFF0000u); }
__device__ __forceinline__ float dot2(unsigned a, unsigned b, float c) {
  return __builtin_amdgcn_fdot2_f32_bf16(__builtin_bit_cast(bf2, a), __builtin_bit_cast(bf2, b), c, false);
}
template <int CTRL> __device__ __forceinline__ float dpp(float x) {
  return __builtin_bit_cast(float, __builtin_amdgcn_mov_dpp(__builtin_bit_cast(int, x), CTRL, 0xf, 0xf, true));
}
constexpr int XOR1 = 0xB1, XOR2 = 0x4E, ROR4 = 0x124, ROR8 = 0x128, SHL4 = 0x104;
__device__ __forceinline__ float xrow_sum(float x) {
  auto s = __builtin_amdgcn_permlane16_swap(__float_as_uint(x), __float_as_uint(x), false, false);
  x = __uint_as_float(s[0]) + __uint_as_float(s[1]);
  auto t = __builtin_amdgcn_permlane32_swap(__float_as_uint(x), __float_as_uint(x), false, false);
  return __uint_as_float(t[0]) + __uint_as_float(t[1]);
}
__device__ __forceinline__ float xrow_max(float x) {
  auto s = __builtin_amdgcn_permlane16_swap(__float_as_uint(x), __float_as_uint(x), false, false);
  x = fmaxf(__uint_as_float(s[0]), __uint_as_float(s[1]));
  auto t = __builtin_amdgcn_permlane32_swap(__float_as_uint(x), __float_as_uint(x), false, false);
  return fmaxf(__uint_as_float(t[0]), __uint_as_float(t[1]));
}
__device__ __forceinline__ float x32_sum(float x) {
  auto t = __builtin_amdgcn_permlane32_swap(__float_as_uint(x), __float_as_uint(x), false, false);
  return __uint_as_float(t[0]) + __uint_as_float(t[1]);
}
__device__ __forceinline__ rsrc_t mkrsrc(const void* p, unsigned bytes) {
  return __builtin_amdgcn_make_buffer_rsrc((void*)p, 0, bytes, 0x00020000);
}
template <int AUX> __device__ __forceinline__ v4u ld128(rsrc_t rs, int voff, int soff) {
  return __builtin_amdgcn_raw_buffer_load_b128(rs, voff, soff, AUX);
}
__device__ __forceinline__ unsigned umax3(unsigned a, unsigned b, unsigned c) { return max(max(a, b), c); }
__device__ __forceinline__ unsigned vmax4(v4u x, unsigned m) { return umax3(x.x, x.y, umax3(x.z, x.w, m)); }

#define DLY 12
__device__ __forceinline__ unsigned long long now() { return __builtin_amdgcn_s_memrealtime(); }
__device__ __forceinline__ void hold(unsigned long long t0, int ticks) {
  if (ticks > 0) while ((long long)(now() - t0) < ticks) __builtin_amdgcn_s_sleep(1);
}
#define GSLEEP 0
#define T_X1_H 75
#define T_WO_X2 275
#define T_GU_X2 145
#define T_Q_O 225
#define T_Q_X3 330
#define T_O_Q 400
#define SETTLE() __builtin_amdgcn_s_sleep(DLY)
template <int N> __device__ __forceinline__ void gather(v4u (&x)[N], rsrc_t rs, const int (&off)[N], bool& dead) {
  for (int spin = 0;; ++spin) {
    asm volatile("" ::: "memory");
#pragma unroll
    for (int i = 0; i < N; ++i) x[i] = ld128<AUX_SC1>(rs, off[i], 0);
    unsigned m = 0u;
#pragma unroll
    for (int i = 0; i < N; ++i) m = vmax4(x[i], m);
    if (!__any(m == SENT)) break;
    if (dead || spin > SPIN_CAP) { dead = true; break; }
    if (GSLEEP) __builtin_amdgcn_s_sleep(GSLEEP);
  }
}
__device__ __forceinline__ unsigned long long epoch_load(rsrc_t rws) {
  const v2u e = __builtin_amdgcn_raw_buffer_load_b64(rws, OFF_EP, 0, AUX_SC1);
  return ((unsigned long long)(unsigned)__builtin_amdgcn_readfirstlane(e.y) << 32 | (unsigned)__builtin_amdgcn_readfirstlane(e.x)) + 1ull;
}
__device__ __forceinline__ void clear16(rsrc_t rws, int off) {
  __builtin_amdgcn_raw_buffer_store_b128(v4u{SENT, SENT, SENT, SENT}, rws, off, 0, AUX_SC1);
}
__device__ __forceinline__ void flag_post(rsrc_t rws, int cu, unsigned long long tag, int l) {
  asm volatile("s_waitcnt vmcnt(0)" ::: "memory");
  if (l == 0) __builtin_amdgcn_raw_buffer_store_b64(v2u{(unsigned)tag, (unsigned)(tag >> 32)}, rws, OFF_FLAG + cu * 8, 0, AUX_SC1);
}
__device__ __forceinline__ void flags_wait(rsrc_t rws, unsigned long long tag, int l, bool& dead) {
  const unsigned lo = (unsigned)tag, hi = (unsigned)(tag >> 32);
  for (int spin = 0;; ++spin) {
    asm volatile("" ::: "memory");
    const v4u a = ld128<AUX_SC1>(rws, OFF_FLAG + l * 32, 0), b = ld128<AUX_SC1>(rws, OFF_FLAG + l * 32 + 16, 0);
    const bool ok = (a.x == lo) & (a.y == hi) & (a.z == lo) & (a.w == hi) & (b.x == lo) & (b.y == hi) & (b.z == lo) & (b.w == hi);
    if (__all(ok)) break;
    if (dead || spin > SPIN_CAP) { dead = true; break; }
    __builtin_amdgcn_s_sleep(4);
  }
}
__device__ __forceinline__ v4u xpose16(v4u raw, v4u sel1, v4u sel2) {
  const f4 z = {0.f, 0.f, 0.f, 0.f};
  const f4 d1 = mfma(raw, sel1, z), d2 = mfma(raw, sel2, z);
  return v4u{hi16pack(d1[1], d1[0]), hi16pack(d1[3], d1[2]), hi16pack(d2[1], d2[0]), hi16pack(d2[3], d2[2])};
}
__device__ __forceinline__ void xpose32(v4u& a, v4u& b, v4u sel1, v4u sel2) {
  const f4 z = {0.f, 0.f, 0.f, 0.f};
  const f4 a1 = mfma(a, sel1, z), b1 = mfma(b, sel1, z), a2 = mfma(a, sel2, z), b2 = mfma(b, sel2, z);
  a = v4u{hi16pack(a1[1], a1[0]), hi16pack(a1[3], a1[2]), hi16pack(b1[1], b1[0]), hi16pack(b1[3], b1[2])};
  b = v4u{hi16pack(a2[1], a2[0]), hi16pack(a2[3], a2[2]), hi16pack(b2[1], b2[0]), hi16pack(b2[3], b2[2])};
}

#define STAMP(i) do { } while (0)
#define STAMP2(p, a, b) do { } while (0)

#define ASM_LD(dst, voff, rs, soff, MODS) \
  asm volatile("buffer_load_dwordx4 %0, %1, %2, %3 offen " MODS : "=v"(dst) : "v"(voff), "s"(rs), "s"(soff) : "memory")
#define ASM_DMA(voff, rs, soff, ldsa) \
  asm volatile("s_mov_b32 m0, %3\n\ts_nop 0\n\tbuffer_load_dwordx4 %0, %1, %2 offen nt lds" ::"v"(voff), "s"(rs), "s"(soff), "s"(ldsa) : "memory")
#define WAIT_VM(n) asm volatile("s_waitcnt vmcnt(" #n ")" ::: "memory")
#define PIN8(a, o) asm volatile("" : "+v"(a[o]), "+v"(a[o + 1]), "+v"(a[o + 2]), "+v"(a[o + 3]), "+v"(a[o + 4]), "+v"(a[o + 5]), "+v"(a[o + 6]), "+v"(a[o + 7]))

__device__ __forceinline__ v2u tr_read(LAS unsigned char* p) {
  s4 t = __builtin_amdgcn_ds_read_tr16_b64_v4i16((LAS s4*)p);
  return __builtin_bit_cast(v2u, t);
}

__device__ __forceinline__ void attn_math(const int PASS, LAS unsigned char* L, const v4u (&kf)[32], const v4u (&vf)[16], const v4u (&qf)[4],
                                          rsrc_t rws, int b, int h, int w, int l_, LAS unsigned* st) {
  int l = l_; asm volatile("" : "+v"(l));
  const int g = l >> 4;
  const f4 z = {0.f, 0.f, 0.f, 0.f};
  constexpr float CS = 0.08838834764831845f * 1.4426950408889634f;
  float s0 = 0.f, s1 = 0.f;
#pragma unroll
  for (int t = 0; t < 8; t += 2) {
    f4 d0 = z, d1 = z;
#pragma unroll
    for (int s = 0; s < 4; ++s) { d0 = mfma(qf[s], kf[t * 4 + s], d0); d1 = mfma(qf[s], kf[t * 4 + 4 + s], d1); }
    if (t < 4) { s0 = (g == t) ? d0[0] : s0; s0 = (g == t + 1) ? d1[0] : s0; }
    else { s1 = (g == t - 4) ? d0[0] : s1; s1 = (g == t - 3) ? d1[0] : s1; }
  }
  STAMP2(PASS, 16, 8);
  float m = fmaxf(s0, s1);
  m = fmaxf(m, dpp<XOR1>(m)); m = fmaxf(m, dpp<XOR2>(m)); m = fmaxf(m, dpp<ROR4>(m)); m = fmaxf(m, dpp<ROR8>(m));
  m = xrow_max(m);
  const __bf16 p0 = (__bf16)__builtin_amdgcn_exp2f((s0 - m) * CS), p1 = (__bf16)__builtin_amdgcn_exp2f((s1 - m) * CS);
  LAS __bf16* pw = (LAS __bf16*)(L + L_P + w * 256);
  pw[l] = p0; pw[64 + l] = p1;
  v4u pf[4];
#pragma unroll
  for (int s = 0; s < 4; ++s) pf[s] = *(LAS v4u*)(L + L_P + w * 256 + s * 64 + g * 16);
  STAMP2(PASS, 17, 9);
  float ls = (float)p0 + (float)p1;
  ls += dpp<XOR1>(ls); ls += dpp<XOR2>(ls); ls += dpp<ROR4>(ls); ls += dpp<ROR8>(ls);
  ls = xrow_sum(ls);
  float oa0 = 0.f, oa1 = 0.f;
#pragma unroll
  for (int n = 0; n < 8; n += 2) {
    f4 d0 = mfma(pf[0], vf[n], z), d1 = mfma(pf[0], vf[n + 1], z);
    d0 = mfma(pf[1], vf[8 + n], d0); d1 = mfma(pf[1], vf[9 + n], d1);
    if (n < 4) { oa0 = (g == n) ? d0[0] : oa0; oa0 = (g == n + 1) ? d1[0] : oa0; }
    else { oa1 = (g == n - 4) ? d0[0] : oa1; oa1 = (g == n - 3) ? d1[0] : oa1; }
  }
  const int q4 = (l >> 2) & 3;
  const int trb = L_V + w * 16384 + (8 * g + q4) * 256 + ((q4 | ((g & 1) << 2)) << 5) + (l & 3) * 8;
  float ob0 = 0.f, ob1 = 0.f;
#pragma unroll
  for (int hb = 0; hb < 2; ++hb) {
    if (hb == 0) WAIT_VM(8); else WAIT_VM(0);
    if (hb == 1) STAMP2(PASS, 2, 6);
    v2u t0[4], t1[4];
    float pc0 = 0.f, pc1 = 0.f;
#define TR_LOAD(n) do { LAS unsigned char* a_ = L + (trb ^ ((n) << 5)) + hb * 8192; t0[(n) & 3] = tr_read(a_); t1[(n) & 3] = tr_read(a_ + 1024); } while (0)
    TR_LOAD(0); TR_LOAD(1);
#pragma unroll
    for (int n = 0; n < 8; n += 2) {
      if (n < 6) { TR_LOAD(n + 2); TR_LOAD(n + 3); }
      __builtin_amdgcn_sched_barrier(0);
      const f4 d0 = mfma(pf[2 + hb], v4u{t0[n & 3].x, t0[n & 3].y, t1[n & 3].x, t1[n & 3].y}, z);
      const f4 d1 = mfma(pf[2 + hb], v4u{t0[(n + 1) & 3].x, t0[(n + 1) & 3].y, t1[(n + 1) & 3].x, t1[(n + 1) & 3].y}, z);
      if (n < 4) { pc0 = (g == n) ? d0[0] : pc0; pc0 = (g == n + 1) ? d1[0] : pc0; }
      else { pc1 = (g == n - 4) ? d0[0] : pc1; pc1 = (g == n - 3) ? d1[0] : pc1; }
    }
    ob0 += pc0; ob1 += pc1;
  }
  LAS float* ow = (LAS float*)(L + L_OW);
  ow[w * 128 + l] = oa0 + ob0; ow[w * 128 + 64 + l] = oa1 + ob1;
  if (l == 0) *(LAS f2*)(L + L_ML + w * 8) = f2{m, ls};
  STAMP2(PASS, 18, 10);
  __syncthreads();
  STAMP2(PASS, 19, 11);
  if (w == 0) {
    f4 ml[4];
    f2 ov[8];
#pragma unroll
    for (int i = 0; i < 4; ++i) ml[i] = *(LAS f4*)(L + L_ML + i * 16);
#pragma unroll
    for (int i = 0; i < 8; ++i) ov[i] = *(LAS f2*)(L + L_OW + i * 512 + l * 8);
    float M = fmaxf(fmaxf(fmaxf(ml[0][0], ml[0][2]), fmaxf(ml[1][0], ml[1][2])), fmaxf(fmaxf(ml[2][0], ml[2][2]), fmaxf(ml[3][0], ml[3][2])));
    float Ls = 0.f, o0 = 0.f, o1 = 0.f;
#pragma unroll
    for (int i = 0; i < 8; ++i) {
      const float f = __builtin_amdgcn_exp2f((ml[i >> 1][(i & 1) * 2] - M) * CS);
      Ls = fmaf(f, ml[i >> 1][(i & 1) * 2 + 1], Ls); o0 = fmaf(f, ov[i].x, o0); o1 = fmaf(f, ov[i].y, o1);
    }
    const float inv = __builtin_amdgcn_rcpf(Ls);
    __builtin_amdgcn_raw_buffer_store_b32(pack_bf16(o0 * inv, o1 * inv), rws, OFF_O + PASS * SZ_O + (h * 4 + b) * 256 + l * 4, 0, AUX_SC1);
  }
  STAMP2(PASS, 3, 7);
}

__device__ __forceinline__ void attn_role(LAS unsigned char* L, const u16* K, const u16* V, rsrc_t rws, int cu, int xc, int y, int w, int l,
                                          LAS unsigned* st) {
  const int h = 4 * xc + (y >> 2), b = y & 3, g = l >> 4, r = l & 15;
  const size_t kv = ((size_t)(b * NH + h) * SK + (SK - WIN) + 128 * w) * DH;
  const rsrc_t rK = mkrsrc(K + kv, 32768), rV = mkrsrc(V + kv, 32768);
  constexpr int PSTR = 8192;
  bool dead = false;
#define SVC_AT 3
#define SVC(n) do { if (SVC_AT == (n) && w == 0) { \
      flags_wait(rws, tag, l, dead); \
      v4u q1[1]; const int off[1] = {l < 16 ? OFF_Q + (h * 16 + b) * 64 + (l >> 2) * 256 + (l & 3) * 16 : OOB}; \
      gather<1>(q1, rws, off, dead); \
      if (l < 16) *(LAS v4u*)(L + L_Q + l * 16) = q1[0]; \
      asm volatile("s_waitcnt lgkmcnt(0)" ::: "memory"); \
      if (l == 0) *(volatile LAS unsigned*)(L + L_QF) = 1u; } } while (0)
  v4u kf[32], vf[16], qf[4];
  const int ldsV = L_V + w * 16384;
  const int ks0 = g * 256 + ((r ^ g) << 4);
#define KX(i) (((i) & 3) << 6)
  const int vs0 = g * 256 + (((r >> 1) ^ g) << 5) + (l & 1) * 16, vs1 = g * 256 + (((r >> 1) ^ (g | 4)) << 5) + (l & 1) * 16;
#define K_PIECE(n) _Pragma("unroll") for (int i = 0; i < 8; ++i) \
    ASM_DMA(ks0 ^ KX(i), rK, (n) * PSTR + i * 1024, ldsV + ((n) & 1) * 8192 + i * 1024)
#define V_PIECE(n, buf) _Pragma("unroll") for (int i = 0; i < 8; ++i) \
    ASM_DMA((i & 2) ? vs1 : vs0, rV, (n) * PSTR + i * 1024, ldsV + (buf) * 8192 + i * 1024)
#define K_READ(n) do { _Pragma("unroll") for (int t = 0; t < 2; ++t) _Pragma("unroll") for (int s = 0; s < 4; ++s) \
      kf[((n) * 2 + t) * 4 + s] = *(LAS v4u*)(L + ldsV + ((n) & 1) * 8192 + (16 * t + r) * 256 + (((4 * s + g) ^ r) << 4)); \
    asm volatile("s_waitcnt lgkmcnt(0)" ::: "memory"); PIN8(kf, (n) * 8); } while (0)
  const int trb = ldsV + (8 * g + ((l >> 2) & 3)) * 256 + ((((l >> 2) & 3) | ((g & 1) << 2)) << 5) + (l & 3) * 8;
#define V_READ(n) do { _Pragma("unroll") for (int c8 = 0; c8 < 8; ++c8) { \
      LAS unsigned char* a = L + (trb ^ (c8 << 5)) + (n) * 8192; \
      const v2u t0 = tr_read(a), t1 = tr_read(a + 1024); \
      vf[(n) * 8 + c8] = v4u{t0.x, t0.y, t1.x, t1.y}; } \
    asm volatile("s_waitcnt lgkmcnt(0)" ::: "memory"); PIN8(vf, (n) * 8); } while (0)
  K_PIECE(0);
  if (threadIdx.x == 0) *(volatile LAS unsigned*)(L + L_QF) = 0u;
  asm volatile("s_waitcnt lgkmcnt(0)\n\ts_barrier" ::: "memory");
  v2u ep = {0u, 0u};
  unsigned long long tag = 0;
  {
    const v4u sent = {SENT, SENT, SENT, SENT};
    const int co = l < 32 ? OFF_O + (l >> 4) * SZ_O + (h * 4 + b) * 256 + r * 16 : OOB;
    asm volatile("s_cmp_eq_u32 %5, 0\n\ts_cbranch_scc0 1f\n\tbuffer_load_dwordx2 %0, %1, %4, 0 offen sc1\n\t"
                 "buffer_store_dwordx4 %2, %3, %4, 0 offen sc1\n\ts_nop 1\n1:"
                 : "+v"(ep) : "v"(OFF_EP), "v"(sent), "v"(co), "s"(rws), "s"(w) : "memory", "scc");
  }
  K_PIECE(1);
  WAIT_VM(8);
  asm volatile("" : "+v"(ep));
  if (w == 0) {
    tag = ((unsigned long long)(unsigned)__builtin_amdgcn_readfirstlane(ep.y) << 32 | (unsigned)__builtin_amdgcn_readfirstlane(ep.x)) + 1ull;
    const v2u tg = {(unsigned)tag, (unsigned)(tag >> 32)};
    asm volatile("buffer_store_dwordx2 %0, %1, %2, 0 offen sc1\n\ts_nop 0" ::"v"(tg), "v"(l == 0 ? OFF_FLAG + cu * 8 : OOB), "s"(rws) : "memory");
  }
  K_READ(0); K_PIECE(2);
  WAIT_VM(8); K_READ(1); K_PIECE(3); SVC(1);
  WAIT_VM(8); K_READ(2); V_PIECE(0, 0); SVC(2);
  WAIT_VM(8); K_READ(3); V_PIECE(1, 1); SVC(3);
  WAIT_VM(8); V_READ(0); V_PIECE(2, 0); SVC(4);
  WAIT_VM(8); V_READ(1); V_PIECE(3, 1); SVC(5);
  STAMP(1);
#pragma unroll 1
  for (int pass = 0; pass < 2; ++pass) {
    if (pass == 0) {
      for (int spin = 0; *(volatile LAS unsigned*)(L + L_QF) == 0u; ++spin) {
        if (spin > (SPIN_CAP << 4)) { dead = true; break; }
        __builtin_amdgcn_s_sleep(1);
      }
#pragma unroll
      for (int s = 0; s < 4; ++s) qf[s] = *(LAS v4u*)(L + L_Q + s * 64 + g * 16);
      asm volatile("" ::: "memory");
    } else {
      hold(now(), T_O_Q);
      int lo = l; asm volatile("" : "+v"(lo));
      const int qo = OFF_Q + SZ_Q + (h * 16 + b) * 64 + (lo >> 4) * 16;
      const int off[4] = {qo, qo + 256, qo + 512, qo + 768};
      gather<4>(qf, rws, off, dead);
    }
    STAMP2(pass, 4, 5);
    attn_math(pass, L, kf, vf, qf, rws, b, h, w, l, st);
  }
}

__device__ __forceinline__ f4 red_sum(LAS unsigned char* L, int row, int l) {
  const int hf = l >> 5, cg = l & 7;
  f4 s = *(LAS f4*)(L + L_RED + (hf * 4) * RED_W + row * 144 + cg * 16);
#pragma unroll
  for (int i = 1; i < 4; ++i) s += *(LAS f4*)(L + L_RED + (hf * 4 + i) * RED_W + row * 144 + cg * 16);
  return f4{x32_sum(s[0]), x32_sum(s[1]), x32_sum(s[2]), x32_sum(s[3])};
}
__device__ __forceinline__ f2 red_stat(LAS unsigned char* L, int row, int l) {
  const int hf = l >> 5;
  f2 s = *(LAS f2*)(L + L_RED + (hf * 4) * RED_W + row * 144 + 128);
#pragma unroll
  for (int i = 1; i < 4; ++i) s += *(LAS f2*)(L + L_RED + (hf * 4 + i) * RED_W + row * 144 + 128);
  return f2{x32_sum(s[0]), x32_sum(s[1])};
}

__device__ __forceinline__ f4 red_sum_gu(LAS unsigned char* L, int row, int l) {
  const int m = l >> 5, cg = l & 7;
  f4 s = *(LAS f4*)(L + L_RED + m * RED_W + row * 144 + cg * 16);
#pragma unroll
  for (int i = 1; i < 4; ++i) s += *(LAS f4*)(L + L_RED + (2 * i + m) * RED_W + row * 144 + cg * 16);
  return s;
}
struct WArgs { const u16 *x, *wq, *wo, *wg, *wu, *wd; u16* out; };

template <int PASS>
__device__ __forceinline__ void q_phase(LAS unsigned char* L, const v4u (&xf)[3], const v4u (&wqf)[6], f4 csq, rsrc_t rws, int tq, int w, int l, LAS unsigned* st) {
  const int g = l >> 4, c = l & 15;
  const f4 z = {0.f, 0.f, 0.f, 0.f};
  const v4u ones = {0x3F803F80u, 0x3F803F80u, 0x3F803F80u, 0x3F803F80u};
  f4 d0 = z, d1 = z, ds = z;
  float ss = 0.f;
#pragma unroll
  for (int i = 0; i < 3; ++i) {
    d0 = mfma(wqf[2 * i], xf[i], d0); d1 = mfma(wqf[2 * i + 1], xf[i], d1); ds = mfma(ones, xf[i], ds);
    ss = dot2(xf[i].x, xf[i].x, ss); ss = dot2(xf[i].y, xf[i].y, ss); ss = dot2(xf[i].z, xf[i].z, ss); ss = dot2(xf[i].w, xf[i].w, ss);
  }
  ss = xrow_sum(ss);
  LAS unsigned char* R = L + L_RED + w * RED_W + c * 144;
  if (c < 4) {
    *(LAS f4*)(R + g * 16) = d0; *(LAS f4*)(R + 64 + g * 16) = d1;
    if (g == 0) *(LAS f2*)(R + 128) = f2{ds[0], ss};
  }
  __syncthreads();
  if (PASS == 1) STAMP(22);
  if (w == 0) {
    const int b = (l >> 3) & 3, cg = l & 7;
    const f4 p = red_sum(L, b, l);
    const f2 s = red_stat(L, b, l);
    const float mu = s[0] * (1.0f / D), var = fmaxf(s[1] * (1.0f / D) - mu * mu, 0.f), rs = __builtin_amdgcn_rsqf(var + 1e-5f);
    if (l < 32) {
      const v2u o = {pack_bf16((p[0] - mu * csq[0]) * rs, (p[1] - mu * csq[1]) * rs), pack_bf16((p[2] - mu * csq[2]) * rs, (p[3] - mu * csq[3]) * rs)};
      __builtin_amdgcn_raw_buffer_store_b64(o, rws, OFF_Q + PASS * SZ_Q + tq * 256 + b * 64 + cg * 8, 0, AUX_SC1);
    }
  }
  __syncthreads();
}

__device__ __forceinline__ void q_second(LAS unsigned char* L, const v4u (&wqf)[6], f4 csq, rsrc_t rws, int tq, int w, int l, bool& dead,
                                         LAS unsigned* st) {
  const int g = l >> 4, c = l & 15;
  v4u xf[3];
  int off[3];
#pragma unroll
  for (int i = 0; i < 3; ++i) {
    const int s = w + 8 * i;
    off[i] = (c < 4 && s < 20) ? OFF_X2 + ((2 * s + (g >> 1)) * 4 + c) * 32 + (g & 1) * 16 : OOB;
  }
  gather<3>(xf, rws, off, dead);
  STAMP(6);
  q_phase<1>(L, xf, wqf, csq, rws, tq, w, l, st);
  STAMP(7);
}

__device__ __forceinline__ void weight_role(LAS unsigned char* L, const WArgs& a, rsrc_t rws, int cu, int xc, int u, int w, int l,
                                            LAS unsigned* st) {
  const int g = l >> 4, r = l & 15, c = r;
  const f4 z = {0.f, 0.f, 0.f, 0.f};
  const v4u ones = {0x3F803F80u, 0x3F803F80u, 0x3F803F80u, 0x3F803F80u};
  v4u sel1, sel2;
  {
    const int j = r - 8 * (g & 1);
    const unsigned one = (j & 1) ? 0x3F800000u : 0x00003F80u;
    const bool in = (unsigned)j < 8u;
    v4u s = {(in && (j >> 1) == 0) ? one : 0u, (in && (j >> 1) == 1) ? one : 0u, (in && (j >> 1) == 2) ? one : 0u, (in && (j >> 1) == 3) ? one : 0u};
    const v4u zz = {0u, 0u, 0u, 0u};
    sel1 = g < 2 ? s : zz; sel2 = g < 2 ? zz : s;
  }
  const int tq = xc * 16 + u;
  const bool has_wo = u < 10, has_gu = u < (xc < 3 ? 8 : 6), has_dn = u >= 10 && u < 15;
  const int wo_id = xc * 10 + u, kq = wo_id / 20, nt32 = wo_id % 20;
  const int t32 = xc < 3 ? xc * 8 + u : 24 + (xc - 3) * 6 + u;
  const int td = xc * 5 + (u - 10);
  const int rho16 = 8 * (r >> 2) + (r & 3);
  const int rho32 = rho16 + 4 * (g >> 1);

  bool dead = false;
  const unsigned long long tstart = __builtin_amdgcn_s_memrealtime();
  v2u ep = {0u, 0u};
  if (w == 0) {
    ep = __builtin_amdgcn_raw_buffer_load_b64(rws, OFF_EP, 0, AUX_SC1);
    const int hf = l >> 5, k = l & 31;
    clear16(rws, k < 16 ? OFF_Q + hf * SZ_Q + tq * 256 + k * 16 : has_wo ? OFF_X1 + hf * SZ_X1 + (kq * 40 + 2 * nt32) * 128 + (k - 16) * 16 : OOB);
    clear16(rws, (k < 16 && has_gu) ? OFF_H + hf * SZ_H + t32 * 256 + k * 16 : (k < 8 && hf == 0 && has_dn) ? OFF_X2 + td * 128 + k * 16 : OOB);
  }
  v4u wqf[6];
  unsigned long long tag = 0;
  f4 csq;
  {
    v4u xf[3];
    const rsrc_t rx = mkrsrc(a.x, NB * D * 2), rq = mkrsrc(a.wq, D * HD * 2);
#pragma unroll
    for (int i = 0; i < 3; ++i) {
      const int s = w + 8 * i;
      xf[i] = ld128<0>(rx, (c < 4 && s < 20) ? c * 1280 + s * 64 + g * 16 : OOB, 0);
    }
#pragma unroll
    for (int i = 0; i < 3; ++i) {
      const int s = w + 8 * i;
      const int vo = s < 20 ? (32 * s + rho16) * 8192 + tq * 64 + g * 16 : OOB;
      wqf[2 * i] = ld128<AUX_NT>(rq, vo, 0); wqf[2 * i + 1] = ld128<AUX_NT>(rq, vo, 4 * 8192);
    }
    __builtin_amdgcn_sched_barrier(0);
    if (w == 0) {
      asm volatile("s_waitcnt vmcnt(9)" : "+v"(ep)::"memory");
      tag = ((unsigned long long)(unsigned)__builtin_amdgcn_readfirstlane(ep.y) << 32 | (unsigned)__builtin_amdgcn_readfirstlane(ep.x)) + 1ull;
      if (l == 0) __builtin_amdgcn_raw_buffer_store_b64(v2u{(unsigned)tag, (unsigned)(tag >> 32)}, rws, OFF_FLAG + cu * 8, 0, AUX_SC1);
    }
#pragma unroll
    for (int i = 0; i < 3; ++i) xpose32(wqf[2 * i], wqf[2 * i + 1], sel1, sel2);
    f4 c0 = z, c1 = z;
#pragma unroll
    for (int i = 0; i < 3; ++i) { c0 = mfma(wqf[2 * i], ones, c0); c1 = mfma(wqf[2 * i + 1], ones, c1); }
    LAS unsigned char* R4 = L + L_RED + w * RED_W + 4 * 144;
    if (c == 0) { *(LAS f4*)(R4 + g * 16) = c0; *(LAS f4*)(R4 + 64 + g * 16) = c1; }
    __syncthreads();
    csq = red_sum(L, 4, l);
    q_phase<0>(L, xf, wqf, csq, rws, tq, w, l, st);
  }
  STAMP(1);
  const int fb = (l >> 3) & 3, fcg = l & 7;
  LAS unsigned char* R = L + L_RED + w * RED_W + c * 144;
#define HOLD() do { while ((long long)(__builtin_amdgcn_s_memrealtime() - tstart) < HOLD_T) __builtin_amdgcn_s_sleep(16); } while (0)

  if (has_wo) {
    v4u wof[8], guf[10];
    const int gm = w & 1, gq = w >> 1;
    {
      const rsrc_t ro = mkrsrc(a.wo, D * HD * 2), rgu = mkrsrc(gm ? a.wu : a.wg, D * FF * 2);
      const int vo = ((8 * kq + w) * 128 + rho16) * 1280 + nt32 * 64 + g * 16;
#pragma unroll
      for (int s = 0; s < 4; ++s) { wof[2 * s] = ld128<AUX_NT>(ro, vo, s * 32 * 1280); wof[2 * s + 1] = ld128<AUX_NT>(ro, vo, (s * 32 + 4) * 1280); }
      const int vg = has_gu ? (160 * gq + rho16) * 3456 + t32 * 64 + g * 16 : OOB;
#pragma unroll
      for (int i = 0; i < 5; ++i) { guf[2 * i] = ld128<AUX_NT>(rgu, vg, i * 32 * 3456); guf[2 * i + 1] = ld128<AUX_NT>(rgu, vg, (i * 32 + 4) * 3456); }
      __builtin_amdgcn_sched_barrier(0);
    }
#pragma unroll
    for (int s = 0; s < 4; ++s) xpose32(wof[2 * s], wof[2 * s + 1], sel1, sel2);
#pragma unroll
    for (int i = 0; i < 5; ++i) xpose32(guf[2 * i], guf[2 * i + 1], sel1, sel2);
    f4 csg;
    {
      f4 c0 = z, c1 = z;
#pragma unroll
      for (int i = 0; i < 5; ++i) { c0 = mfma(guf[2 * i], ones, c0); c1 = mfma(guf[2 * i + 1], ones, c1); }
      LAS unsigned char* R4 = L + L_RED + w * RED_W + 4 * 144;
      if (c == 0) { *(LAS f4*)(R4 + g * 16) = c0; *(LAS f4*)(R4 + 64 + g * 16) = c1; }
      if (w == 0) flags_wait(rws, tag, l, dead);
      __syncthreads();
      csg = red_sum_gu(L, 4, l);
    }
    STAMP(2);
    HOLD();
#pragma unroll 1
    for (int pass = 0; pass < 2; ++pass) {
      {
        v4u of[4];
        int lo = l; asm volatile("" : "+v"(lo));
        const int o0 = (lo & 15) < 4 ? OFF_O + pass * SZ_O + ((8 * kq + w) * 4 + (lo & 15)) * 256 + (lo >> 4) * 16 : OOB;
        const int off[4] = {o0, o0 + 64, o0 + 128, o0 + 192};
        v2u res = {0u, 0u};
        if (w == 0 && kq == 0 && l < 32) {
          if (pass == 0) res = *(const v2u*)(a.x + fb * D + nt32 * 32 + fcg * 4);
          else res = __builtin_amdgcn_raw_buffer_load_b64(rws, OFF_X2 + ((2 * nt32 + (fcg >> 2)) * 4 + fb) * 32 + (fcg & 3) * 8, 0, AUX_SC1);
        }
        gather<4>(of, rws, off, dead);
        STAMP2(pass, 3, 9);
        f4 d0 = z, d1 = z;
#pragma unroll
        for (int s = 0; s < 4; ++s) { d0 = mfma(wof[2 * s], of[s], d0); d1 = mfma(wof[2 * s + 1], of[s], d1); }
        if (c < 4) { *(LAS f4*)(R + g * 16) = d0; *(LAS f4*)(R + 64 + g * 16) = d1; }
        __syncthreads();
        STAMP2(pass, 16, 24);
        if (w == 0) {
          f4 p = red_sum(L, fb, l);
          p[0] += bflo(res.x); p[1] += bfhi(res.x); p[2] += bflo(res.y); p[3] += bfhi(res.y);
          if (l < 32) {
            const v2u o = {pack_bf16(p[0], p[1]), pack_bf16(p[2], p[3])};
            __builtin_amdgcn_raw_buffer_store_b64(o, rws, OFF_X1 + pass * SZ_X1 + ((kq * 40 + 2 * nt32 + (fcg >> 2)) * 4 + fb) * 32 + (fcg & 3) * 8, 0, AUX_SC1);
          }
          STAMP2(pass, 17, 25);
        }
        __syncthreads();
      }
      if (has_gu) {
        v4u x1[5];
        int xo[5];
        int lo = l; asm volatile("" : "+v"(lo));
#pragma unroll
        for (int i = 0; i < 5; ++i)
          xo[i] = OFF_X1 + pass * SZ_X1 + ((((lo & 15) >> 2) * 40 + 2 * (5 * gq + i) + (lo >> 5)) * 4 + (lo & 3)) * 32 + ((lo >> 4) & 1) * 16;
        SETTLE();
        gather<5>(x1, rws, xo, dead);
        STAMP2(pass, 4, 10);
        f4 d0 = z, d1 = z, ds = z;
#pragma unroll
        for (int i = 0; i < 5; ++i) { d0 = mfma(guf[2 * i], x1[i], d0); d1 = mfma(guf[2 * i + 1], x1[i], d1); }
        if (gm == 0) {
#pragma unroll
          for (int i = 0; i < 5; ++i) ds = mfma(ones, x1[i], ds);
        }
#pragma unroll
        for (int i = 0; i < 4; ++i) {
          d0[i] += dpp<ROR4>(d0[i]); d0[i] += dpp<ROR8>(d0[i]);
          d1[i] += dpp<ROR4>(d1[i]); d1[i] += dpp<ROR8>(d1[i]);
        }
        float sx = ds[0]; sx += dpp<ROR4>(sx); sx += dpp<ROR8>(sx);
        if (c < 4) {
          *(LAS f4*)(R + g * 16) = d0; *(LAS f4*)(R + 64 + g * 16) = d1;
          if (g == 0) *(LAS float*)(R + 128) = sx;
        }
        __syncthreads();
        STAMP2(pass, 18, 26);
        if (w == 0) {
          const f4 p = red_sum_gu(L, fb, l);
          float sm = 0.f;
#pragma unroll
          for (int i = 0; i < 4; ++i) sm += *(LAS float*)(L + L_RED + 2 * i * RED_W + fb * 144 + 128);
          const float mu = sm * (1.0f / D);
          float hv[4];
#pragma unroll
          for (int i = 0; i < 4; ++i) {
            const float v = p[i] - mu * csg[i];
            auto sw = __builtin_amdgcn_permlane32_swap(__float_as_uint(v), __float_as_uint(v), false, false);
            hv[i] = fmaxf(__uint_as_float(sw[0]), 0.f) * __uint_as_float(sw[1]);
          }
          if (l < 32) {
            const v2u o = {pack_bf16(hv[0], hv[1]), pack_bf16(hv[2], hv[3])};
            __builtin_amdgcn_raw_buffer_store_b64(o, rws, OFF_H + pass * SZ_H + ((2 * t32 + (fcg >> 2)) * 4 + fb) * 32 + (fcg & 3) * 8, 0, AUX_SC1);
          }
          STAMP2(pass, 19, 27);
        }
        __syncthreads();
      }
      if (pass == 0) { hold(now(), has_gu ? T_GU_X2 : T_WO_X2); q_second(L, wqf, csq, rws, tq, w, l, dead, st); hold(now(), T_Q_O); }
    }
  } else {
    v4u wdf[7];
    {
      const rsrc_t rd = mkrsrc(a.wd, D * FF * 2);
#pragma unroll
      for (int i = 0; i < 7; ++i) {
        const int s = w + 8 * i;
        wdf[i] = ld128<AUX_NT>(rd, (has_dn && s < 54) ? (32 * s + rho32) * 1280 + td * 32 + (g & 1) * 16 : OOB, 0);
      }
      __builtin_amdgcn_sched_barrier(0);
    }
#pragma unroll
    for (int i = 0; i < 7; ++i) wdf[i] = xpose16(wdf[i], sel1, sel2);
    if (w == 0) flags_wait(rws, tag, l, dead);
    __syncthreads();
    STAMP(2);
    HOLD();
#pragma unroll 1
    for (int pass = 0; pass < 2; ++pass) {
      if (has_dn) {
        v4u x1[3];
        int xoff[3];
        int lo = l; asm volatile("" : "+v"(lo));
#pragma unroll
        for (int i = 0; i < 3; ++i) {
          const int s = w + 8 * i;
          xoff[i] = s < 20 ? OFF_X1 + pass * SZ_X1 + ((((lo & 15) >> 2) * 40 + 2 * s + (lo >> 5)) * 4 + (lo & 3)) * 32 + ((lo >> 4) & 1) * 16 : OOB;
        }
        gather<3>(x1, rws, xoff, dead);
        const unsigned long long tx1 = now();
        STAMP2(pass, 4, 10);
        f4 gram = z, ds = z;
#pragma unroll
        for (int i = 0; i < 3; ++i) { gram = mfma(x1[i], x1[i], gram); ds = mfma(ones, x1[i], ds); }
        float sq = (c & 3) == 0 ? gram[0] : (c & 3) == 1 ? gram[1] : (c & 3) == 2 ? gram[2] : gram[3];
        sq += dpp<ROR4>(sq); sq += dpp<ROR8>(sq); sq = xrow_sum(sq);
        float sx = ds[0]; sx += dpp<ROR4>(sx); sx += dpp<ROR8>(sx);
        if (c < 4 && g == 0) *(LAS f2*)(R + 128) = f2{sx, sq};
        __syncthreads();
        float rs2 = 0.f; f4 res = z;
        if (w == 0) {
          v2u rp[4];
#pragma unroll
          for (int k = 0; k < 4; ++k)
            rp[k] = __builtin_amdgcn_raw_buffer_load_b64(rws, OFF_X1 + pass * SZ_X1 + ((k * 40 + td) * 4 + fb) * 32 + (fcg & 3) * 8, 0, AUX_SC1);
          const f2 s = red_stat(L, fb, l);
          const float mu = s[0] * (1.0f / D), var = fmaxf(s[1] * (1.0f / D) - mu * mu, 0.f);
          rs2 = __builtin_amdgcn_rcpf(var + 1e-5f);
#pragma unroll
          for (int k = 0; k < 4; ++k) { res[0] += bflo(rp[k].x); res[1] += bfhi(rp[k].x); res[2] += bflo(rp[k].y); res[3] += bfhi(rp[k].y); }
        }
        v4u hf[7];
        int hoff[7];
#pragma unroll
        for (int i = 0; i < 7; ++i) {
          const int s = w + 8 * i;
          hoff[i] = ((lo & 15) < 4 && s < 54) ? OFF_H + pass * SZ_H + ((2 * s + (lo >> 5)) * 4 + (lo & 15)) * 32 + ((lo >> 4) & 1) * 16 : OOB;
        }
        hold(tx1, T_X1_H);
        gather<7>(hf, rws, hoff, dead);
        STAMP2(pass, 5, 11);
        f4 d = z;
#pragma unroll
        for (int i = 0; i < 7; ++i) d = mfma(wdf[i], hf[i], d);
        if (c < 4) { *(LAS f4*)(R + g * 16) = d; *(LAS f4*)(R + 64 + g * 16) = z; }
        __syncthreads();
        STAMP2(pass, 20, 28);
        if (w == 0) {
          const f4 p = red_sum(L, fb, l);
          if (l < 32 && fcg < 4) {
            f4 o;
#pragma unroll
            for (int i = 0; i < 4; ++i) o[i] = res[i] + rs2 * p[i];
            if (dead) o = f4{__builtin_nanf(""), __builtin_nanf(""), __builtin_nanf(""), __builtin_nanf("")};
            const v2u ob = {pack_bf16(o[0], o[1]), pack_bf16(o[2], o[3])};
            if (pass == 0) __builtin_amdgcn_raw_buffer_store_b64(ob, rws, OFF_X2 + (td * 4 + fb) * 32 + fcg * 8, 0, AUX_SC1);
            else *(v2u*)(a.out + fb * D + td * 16 + fcg * 4) = ob;
          }
          STAMP2(pass, 21, 29);
          if (pass == 1 && td == 0 && l == 0) __builtin_amdgcn_raw_buffer_store_b64(v2u{(unsigned)tag, (unsigned)(tag >> 32)}, rws, OFF_EP, 0, AUX_SC1);
        }
        __syncthreads();
      }
      if (pass == 0) { if (has_dn) SETTLE(); q_second(L, wqf, csq, rws, tq, w, l, dead, st); hold(now(), T_Q_X3); }
    }
  }
}

__global__ __launch_bounds__(512, 2) void mega_kernel(const u16* __restrict__ x, const u16* __restrict__ K, const u16* __restrict__ V,
                                                      const u16* __restrict__ wq, const u16* __restrict__ wo, const u16* __restrict__ wg,
                                                      const u16* __restrict__ wu, const u16* __restrict__ wd, u16* __restrict__ out,
                                                      unsigned char* __restrict__ ws) {
  __shared__ __attribute__((aligned(16))) unsigned char lds[L_END];
  LAS unsigned char* L = (LAS unsigned char*)lds;
  const int cu = blockIdx.x, xc = cu & 7, y = cu >> 3;
  const int l = threadIdx.x & 63, w = __builtin_amdgcn_readfirstlane(threadIdx.x >> 6);
  const rsrc_t rws = mkrsrc(ws, WS_USED);
  LAS unsigned* st = (LAS unsigned*)(L + L_ST) + w * 32;
  if (y < 16) attn_role(L, K, V, rws, cu, xc, y, w, l, st);
  else {
    WArgs a{x, wq, wo, wg, wu, wd, out};
    weight_role(L, a, rws, cu, xc, y - 16, w, l, st);
  }
}
}

extern "C" void kernel_launch(void* const* d_in, const int* in_sizes, int n_in, void* d_out, int out_size, void* d_ws, size_t ws_size,
                              hipStream_t stream) {
  (void)in_sizes; (void)n_in; (void)out_size; (void)ws_size;
  mega_kernel<<<256, 512, 0, stream>>>((const u16*)d_in[0], (const u16*)d_in[1], (const u16*)d_in[2], (const u16*)d_in[3],
                                       (const u16*)d_in[4], (const u16*)d_in[6], (const u16*)d_in[7], (const u16*)d_in[8],
                                       (u16*)d_out, (unsigned char*)d_ws);
}
```

```cpp
#include <hip/hip_runtime.h>
#include <stdint.h>

#define MK_STAMPS 0
#define HOLD_T 1280

namespace {
typedef unsigned short u16;
typedef unsigned v4u __attribute__((ext_vector_type(4)));
typedef unsigned v2u __attribute__((ext_vector_type(2)));
typedef float f4 __attribute__((ext_vector_type(4)));
typedef float f2 __attribute__((ext_vector_type(2)));
typedef __bf16 bf8 __attribute__((ext_vector_type(8)));
typedef __bf16 bf2 __attribute__((ext_vector_type(2)));
typedef short s4 __attribute__((ext_vector_type(4)));
typedef __amdgpu_buffer_rsrc_t rsrc_t;
#define LAS __attribute__((address_space(3)))

constexpr int NB = 4, D = 640, NH = 32, DH = 128, SK = 4096, WIN = 1024, FF = 1728, HD = 4096;
constexpr unsigned SENT = 0xFFFFFFFFu;
constexpr int OOB = 0x40000000;
constexpr int SZ_Q = 32 * 4 * 256, SZ_O = 32 * 4 * 256, SZ_X1 = 4 * 40 * 128, SZ_H = 108 * 128, SZ_X2 = 40 * 128;
constexpr int OFF_Q = 0, OFF_O = OFF_Q + 2 * SZ_Q, OFF_X1 = OFF_O + 2 * SZ_O, OFF_H = OFF_X1 + 2 * SZ_X1,
              OFF_X2 = OFF_H + 2 * SZ_H, OFF_FLAG = OFF_X2 + SZ_X2, OFF_EP = OFF_FLAG + 2048, WS_USED = OFF_EP + 128;
constexpr int OFF_STAMP = 1 << 20;
constexpr int SPIN_CAP = 1 << 17;
constexpr int L_V = 0;
constexpr int L_P = 131072;
constexpr int L_OW = L_P + 2048;
constexpr int L_ML = L_OW + 4096;
constexpr int L_Q = L_ML + 64;
constexpr int L_QF = L_Q + 256;
constexpr int L_ST = L_QF + 16;
constexpr int L_END = L_ST + (MK_STAMPS ? 1024 : 0);
constexpr int L_RED = 0;
constexpr int RED_W = 5 * 36 * 4;

constexpr int AUX_NT = 2, AUX_SC1 = 16;

__device__ __forceinline__ f4 mfma(v4u a, v4u b, f4 c) {
  return __builtin_amdgcn_mfma_f32_16x16x32_bf16(__builtin_bit_cast(bf8, a), __builtin_bit_cast(bf8, b), c, 0, 0, 0);
}
__device__ __forceinline__ unsigned hi16pack(float hi, float lo) {
  return __builtin_amdgcn_perm(__builtin_bit_cast(unsigned, hi), __builtin_bit_cast(unsigned, lo), 0x07060302u);
}
__device__ __forceinline__ unsigned pack_bf16(float lo, float hi) {
  bf2 r = {(__bf16)lo, (__bf16)hi};
  unsigned u = __builtin_bit_cast(unsigned, r);
  return u == SENT ? 0x7FFF7FFFu : u;
}
__device__ __forceinline__ float bflo(unsigned u) { return __builtin_bit_cast(float, u << 16); }
__device__ __forceinline__ float bfhi(unsigned u) { return __builtin_bit_cast(float, u & 0xFFFF0000u); }
__device__ __forceinline__ float dot2(unsigned a, unsigned b, float c) {
  return __builtin_amdgcn_fdot2_f32_bf16(__builtin_bit_cast(bf2, a), __builtin_bit_cast(bf2, b), c, false);
}
template <int CTRL> __device__ __forceinline__ float dpp(float x) {
  return __builtin_bit_cast(float, __builtin_amdgcn_mov_dpp(__builtin_bit_cast(int, x), CTRL, 0xf, 0xf, true));
}
constexpr int XOR1 = 0xB1, XOR2 = 0x4E, ROR4 = 0x124, ROR8 = 0x128, SHL4 = 0x104;
__device__ __forceinline__ float xrow_sum(float x) {
  auto s = __builtin_amdgcn_permlane16_swap(__float_as_uint(x), __float_as_uint(x), false, false);
  x = __uint_as_float(s[0]) + __uint_as_float(s[1]);
  auto t = __builtin_amdgcn_permlane32_swap(__float_as_uint(x), __float_as_uint(x), false, false);
  return __uint_as_float(t[0]) + __uint_as_float(t[1]);
}
__device__ __forceinline__ float xrow_max(float x) {
  auto s = __builtin_amdgcn_permlane16_swap(__float_as_uint(x), __float_as_uint(x), false, false);
  x = fmaxf(__uint_as_float(s[0]), __uint_as_float(s[1]));
  auto t = __builtin_amdgcn_permlane32_swap(__float_as_uint(x), __float_as_uint(x), false, false);
  return fmaxf(__uint_as_float(t[0]), __uint_as_float(t[1]));
}
__device__ __forceinline__ float x32_sum(float x) {
  auto t = __builtin_amdgcn_permlane32_swap(__float_as_uint(x), __float_as_uint(x), false, false);
  return __uint_as_float(t[0]) + __uint_as_float(t[1]);
}
__device__ __forceinline__ rsrc_t mkrsrc(const void* p, unsigned bytes) {
  return __builtin_amdgcn_make_buffer_rsrc((void*)p, 0, bytes, 0x00020000);
}
template <int AUX> __device__ __forceinline__ v4u ld128(rsrc_t rs, int voff, int soff) {
  return __builtin_amdgcn_raw_buffer_load_b128(rs, voff, soff, AUX);
}
__device__ __forceinline__ unsigned umax3(unsigned a, unsigned b, unsigned c) { return max(max(a, b), c); }
__device__ __forceinline__ unsigned vmax4(v4u x, unsigned m) { return umax3(x.x, x.y, umax3(x.z, x.w, m)); }

#define DLY 12
__device__ __forceinline__ unsigned long long now() { return __builtin_amdgcn_s_memrealtime(); }
__device__ __forceinline__ void hold(unsigned long long t0, int ticks) {
  if (ticks > 0) while ((long long)(now() - t0) < ticks) __builtin_amdgcn_s_sleep(1);
}
#define GSLEEP 0
#define T_X1_H 85
#define T_WO_X2 290
#define T_GU_X2 155
#define T_Q_O 235
#define T_Q_X3 330
#define T_O_Q 450
#define SETTLE() __builtin_amdgcn_s_sleep(DLY)
template <int N> __device__ __forceinline__ void gather(v4u (&x)[N], rsrc_t rs, const int (&off)[N], bool& dead) {
  for (int spin = 0;; ++spin) {
    asm volatile("" ::: "memory");
#pragma unroll
    for (int i = 0; i < N; ++i) x[i] = ld128<AUX_SC1>(rs, off[i], 0);
    unsigned m = 0u;
#pragma unroll
    for (int i = 0; i < N; ++i) m = vmax4(x[i], m);
    if (!__any(m == SENT)) break;
    if (dead || spin > SPIN_CAP) { dead = true; break; }
    if (GSLEEP) __builtin_amdgcn_s_sleep(GSLEEP);
  }
}
__device__ __forceinline__ unsigned long long epoch_load(rsrc_t rws) {
  const v2u e = __builtin_amdgcn_raw_buffer_load_b64(rws, OFF_EP, 0, AUX_SC1);
  return ((unsigned long long)(unsigned)__builtin_amdgcn_readfirstlane(e.y) << 32 | (unsigned)__builtin_amdgcn_readfirstlane(e.x)) + 1ull;
}
__device__ __forceinline__ void clear16(rsrc_t rws, int off) {
  __builtin_amdgcn_raw_buffer_store_b128(v4u{SENT, SENT, SENT, SENT}, rws, off, 0, AUX_SC1);
}
__device__ __forceinline__ void flag_post(rsrc_t rws, int cu, unsigned long long tag, int l) {
  asm volatile("s_waitcnt vmcnt(0)" ::: "memory");
  if (l == 0) __builtin_amdgcn_raw_buffer_store_b64(v2u{(unsigned)tag, (unsigned)(tag >> 32)}, rws, OFF_FLAG + cu * 8, 0, AUX_SC1);
}
__device__ __forceinline__ void flags_wait(rsrc_t rws, unsigned long long tag, int l, bool& dead) {
  const unsigned lo = (unsigned)tag, hi = (unsigned)(tag >> 32);
  for (int spin = 0;; ++spin) {
    asm volatile("" ::: "memory");
    const v4u a = ld128<AUX_SC1>(rws, OFF_FLAG + l * 32, 0), b = ld128<AUX_SC1>(rws, OFF_FLAG + l * 32 + 16, 0);
    const bool ok = (a.x == lo) & (a.y == hi) & (a.z == lo) & (a.w == hi) & (b.x == lo) & (b.y == hi) & (b.z == lo) & (b.w == hi);
    if (__all(ok)) break;
    if (dead || spin > SPIN_CAP) { dead = true; break; }
    __builtin_amdgcn_s_sleep(4);
  }
}
__device__ __forceinline__ v4u xpose16(v4u raw, v4u sel1, v4u sel2) {
  const f4 z = {0.f, 0.f, 0.f, 0.f};
  const f4 d1 = mfma(raw, sel1, z), d2 = mfma(raw, sel2, z);
  return v4u{hi16pack(d1[1], d1[0]), hi16pack(d1[3], d1[2]), hi16pack(d2[1], d2[0]), hi16pack(d2[3], d2[2])};
}
__device__ __forceinline__ void xpose32(v4u& a, v4u& b, v4u sel1, v4u sel2) {
  const f4 z = {0.f, 0.f, 0.f, 0.f};
  const f4 a1 = mfma(a, sel1, z), b1 = mfma(b, sel1, z), a2 = mfma(a, sel2, z), b2 = mfma(b, sel2, z);
  a = v4u{hi16pack(a1[1], a1[0]), hi16pack(a1[3], a1[2]), hi16pack(b1[1], b1[0]), hi16pack(b1[3], b1[2])};
  b = v4u{hi16pack(a2[1], a2[0]), hi16pack(a2[3], a2[2]), hi16pack(b2[1], b2[0]), hi16pack(b2[3], b2[2])};
}

#define STAMP(i) do { } while (0)
#define STAMP2(p, a, b) do { } while (0)

#define ASM_LD(dst, voff, rs, soff, MODS) \
  asm volatile("buffer_load_dwordx4 %0, %1, %2, %3 offen " MODS : "=v"(dst) : "v"(voff), "s"(rs), "s"(soff) : "memory")
#define ASM_DMA(voff, rs, soff, ldsa) \
  asm volatile("s_mov_b32 m0, %3\n\ts_nop 0\n\tbuffer_load_dwordx4 %0, %1, %2 offen nt lds" ::"v"(voff), "s"(rs), "s"(soff), "s"(ldsa) : "memory")
#define WAIT_VM(n) asm volatile("s_waitcnt vmcnt(" #n ")" ::: "memory")
#define PIN8(a, o) asm volatile("" : "+v"(a[o]), "+v"(a[o + 1]), "+v"(a[o + 2]), "+v"(a[o + 3]), "+v"(a[o + 4]), "+v"(a[o + 5]), "+v"(a[o + 6]), "+v"(a[o + 7]))

__device__ __forceinline__ v2u tr_read(LAS unsigned char* p) {
  s4 t = __builtin_amdgcn_ds_read_tr16_b64_v4i16((LAS s4*)p);
  return __builtin_bit_cast(v2u, t);
}

__device__ __forceinline__ void attn_math(const int PASS, LAS unsigned char* L, const v4u (&kf)[32], const v4u (&vf)[16], const v4u (&qf)[4],
                                          rsrc_t rws, int b, int h, int w, int l_, LAS unsigned* st) {
  int l = l_; asm volatile("" : "+v"(l));
  const int g = l >> 4;
  const f4 z = {0.f, 0.f, 0.f, 0.f};
  constexpr float CS = 0.08838834764831845f * 1.4426950408889634f;
  float s0 = 0.f, s1 = 0.f;
#pragma unroll
  for (int t = 0; t < 8; t += 2) {
    f4 d0 = z, d1 = z;
#pragma unroll
    for (int s = 0; s < 4; ++s) { d0 = mfma(qf[s], kf[t * 4 + s], d0); d1 = mfma(qf[s], kf[t * 4 + 4 + s], d1); }
    if (t < 4) { s0 = (g == t) ? d0[0] : s0; s0 = (g == t + 1) ? d1[0] : s0; }
    else { s1 = (g == t - 4) ? d0[0] : s1; s1 = (g == t - 3) ? d1[0] : s1; }
  }
  STAMP2(PASS, 16, 8);
  float m = fmaxf(s0, s1);
  m = fmaxf(m, dpp<XOR1>(m)); m = fmaxf(m, dpp<XOR2>(m)); m = fmaxf(m, dpp<ROR4>(m)); m = fmaxf(m, dpp<ROR8>(m));
  m = xrow_max(m);
  const __bf16 p0 = (__bf16)__builtin_amdgcn_exp2f((s0 - m) * CS), p1 = (__bf16)__builtin_amdgcn_exp2f((s1 - m) * CS);
  LAS __bf16* pw = (LAS __bf16*)(L + L_P + w * 256);
  pw[l] = p0; pw[64 + l] = p1;
  v4u pf[4];
#pragma unroll
  for (int s = 0; s < 4; ++s) pf[s] = *(LAS v4u*)(L + L_P + w * 256 + s * 64 + g * 16);
  STAMP2(PASS, 17, 9);
  float ls = (float)p0 + (float)p1;
  ls += dpp<XOR1>(ls); ls += dpp<XOR2>(ls); ls += dpp<ROR4>(ls); ls += dpp<ROR8>(ls);
  ls = xrow_sum(ls);
  float oa0 = 0.f, oa1 = 0.f;
#pragma unroll
  for (int n = 0; n < 8; n += 2) {
    f4 d0 = mfma(pf[0], vf[n], z), d1 = mfma(pf[0], vf[n + 1], z);
    d0 = mfma(pf[1], vf[8 + n], d0); d1 = mfma(pf[1], vf[9 + n], d1);
    if (n < 4) { oa0 = (g == n) ? d0[0] : oa0; oa0 = (g == n + 1) ? d1[0] : oa0; }
    else { oa1 = (g == n - 4) ? d0[0] : oa1; oa1 = (g == n - 3) ? d1[0] : oa1; }
  }
  const int q4 = (l >> 2) & 3;
  const int trb = L_V + w * 16384 + (8 * g + q4) * 256 + ((q4 | ((g & 1) << 2)) << 5) + (l & 3) * 8;
  float ob0 = 0.f, ob1 = 0.f;
#pragma unroll
  for (int hb = 0; hb < 2; ++hb) {
    if (hb == 0) WAIT_VM(8); else WAIT_VM(0);
    if (hb == 1) STAMP2(PASS, 2, 6);
    v2u t0[4], t1[4];
    float pc0 = 0.f, pc1 = 0.f;
#define TR_LOAD(n) do { LAS unsigned char* a_ = L + (trb ^ ((n) << 5)) + hb * 8192; t0[(n) & 3] = tr_read(a_); t1[(n) & 3] = tr_read(a_ + 1024); } while (0)
    TR_LOAD(0); TR_LOAD(1);
#pragma unroll
    for (int n = 0; n < 8; n += 2) {
      if (n < 6) { TR_LOAD(n + 2); TR_LOAD(n + 3); }
      __builtin_amdgcn_sched_barrier(0);
      const f4 d0 = mfma(pf[2 + hb], v4u{t0[n & 3].x, t0[n & 3].y, t1[n & 3].x, t1[n & 3].y}, z);
      const f4 d1 = mfma(pf[2 + hb], v4u{t0[(n + 1) & 3].x, t0[(n + 1) & 3].y, t1[(n + 1) & 3].x, t1[(n + 1) & 3].y}, z);
      if (n < 4) { pc0 = (g == n) ? d0[0] : pc0; pc0 = (g == n + 1) ? d1[0] : pc0; }
      else { pc1 = (g == n - 4) ? d0[0] : pc1; pc1 = (g == n - 3) ? d1[0] : pc1; }
    }
    ob0 += pc0; ob1 += pc1;
  }
  LAS float* ow = (LAS float*)(L + L_OW);
  ow[w * 128 + l] = oa0 + ob0; ow[w * 128 + 64 + l] = oa1 + ob1;
  if (l == 0) *(LAS f2*)(L + L_ML + w * 8) = f2{m, ls};
  STAMP2(PASS, 18, 10);
  __syncthreads();
  STAMP2(PASS, 19, 11);
  if (w == 0) {
    f4 ml[4];
    f2 ov[8];
#pragma unroll
    for (int i = 0; i < 4; ++i) ml[i] = *(LAS f4*)(L + L_ML + i * 16);
#pragma unroll
    for (int i = 0; i < 8; ++i) ov[i] = *(LAS f2*)(L + L_OW + i * 512 + l * 8);
    float M = fmaxf(fmaxf(fmaxf(ml[0][0], ml[0][2]), fmaxf(ml[1][0], ml[1][2])), fmaxf(fmaxf(ml[2][0], ml[2][2]), fmaxf(ml[3][0], ml[3][2])));
    float Ls = 0.f, o0 = 0.f, o1 = 0.f;
#pragma unroll
    for (int i = 0; i < 8; ++i) {
      const float f = __builtin_amdgcn_exp2f((ml[i >> 1][(i & 1) * 2] - M) * CS);
      Ls = fmaf(f, ml[i >> 1][(i & 1) * 2 + 1], Ls); o0 = fmaf(f, ov[i].x, o0); o1 = fmaf(f, ov[i].y, o1);
    }
    const float inv = __builtin_amdgcn_rcpf(Ls);
    __builtin_amdgcn_raw_buffer_store_b32(pack_bf16(o0 * inv, o1 * inv), rws, OFF_O + PASS * SZ_O + (h * 4 + b) * 256 + l * 4, 0, AUX_SC1);
  }
  STAMP2(PASS, 3, 7);
}

__device__ __forceinline__ void attn_role(LAS unsigned char* L, const u16* K, const u16* V, rsrc_t rws, int cu, int xc, int y, int w, int l,
                                          LAS unsigned* st) {
  const int h = 4 * xc + (y >> 2), b = y & 3, g = l >> 4, r = l & 15;
  const size_t kv = ((size_t)(b * NH + h) * SK + (SK - WIN) + 128 * w) * DH;
  const rsrc_t rK = mkrsrc(K + kv, 32768), rV = mkrsrc(V + kv, 32768);
  constexpr int PSTR = 8192;
  bool dead = false;
#define SVC_AT 3
#define SVC(n) do { if (SVC_AT == (n) && w == 0) { \
      flags_wait(rws, tag, l, dead); \
      v4u q1[1]; const int off[1] = {l < 16 ? OFF_Q + (h * 16 + b) * 64 + (l >> 2) * 256 + (l & 3) * 16 : OOB}; \
      gather<1>(q1, rws, off, dead); \
      if (l < 16) *(LAS v4u*)(L + L_Q + l * 16) = q1[0]; \
      asm volatile("s_waitcnt lgkmcnt(0)" ::: "memory"); \
      if (l == 0) *(volatile LAS unsigned*)(L + L_QF) = 1u; } } while (0)
  v4u kf[32], vf[16], qf[4];
  const int ldsV = L_V + w * 16384;
  const int ks0 = g * 256 + ((r ^ g) << 4);
#define KX(i) (((i) & 3) << 6)
  const int vs0 = g * 256 + (((r >> 1) ^ g) << 5) + (l & 1) * 16, vs1 = g * 256 + (((r >> 1) ^ (g | 4)) << 5) + (l & 1) * 16;
#define K_PIECE(n) _Pragma("unroll") for (int i = 0; i < 8; ++i) \
    ASM_DMA(ks0 ^ KX(i), rK, (n) * PSTR + i * 1024, ldsV + ((n) & 1) * 8192 + i * 1024)
#define V_PIECE(n, buf) _Pragma("unroll") for (int i = 0; i < 8; ++i) \
    ASM_DMA((i & 2) ? vs1 : vs0, rV, (n) * PSTR + i * 1024, ldsV + (buf) * 8192 + i * 1024)
#define K_READ(n) do { _Pragma("unroll") for (int t = 0; t < 2; ++t) _Pragma("unroll") for (int s = 0; s < 4; ++s) \
      kf[((n) * 2 + t) * 4 + s] = *(LAS v4u*)(L + ldsV + ((n) & 1) * 8192 + (16 * t + r) * 256 + (((4 * s + g) ^ r) << 4)); \
    asm volatile("s_waitcnt lgkmcnt(0)" ::: "memory"); PIN8(kf, (n) * 8); } while (0)
  const int trb = ldsV + (8 * g + ((l >> 2) & 3)) * 256 + ((((l >> 2) & 3) | ((g & 1) << 2)) << 5) + (l & 3) * 8;
#define V_READ(n) do { _Pragma("unroll") for (int c8 = 0; c8 < 8; ++c8) { \
      LAS unsigned char* a = L + (trb ^ (c8 << 5)) + (n) * 8192; \
      const v2u t0 = tr_read(a), t1 = tr_read(a + 1024); \
      vf[(n) * 8 + c8] = v4u{t0.x, t0.y, t1.x, t1.y}; } \
    asm volatile("s_waitcnt lgkmcnt(0)" ::: "memory"); PIN8(vf, (n) * 8); } while (0)
  K_PIECE(0);
  if (threadIdx.x == 0) *(volatile LAS unsigned*)(L + L_QF) = 0u;
  asm volatile("s_waitcnt lgkmcnt(0)\n\ts_barrier" ::: "memory");
  v2u ep = {0u, 0u};
  unsigned long long tag = 0;
  {
    const v4u sent = {SENT, SENT, SENT, SENT};
    const int co = l < 32 ? OFF_O + (l >> 4) * SZ_O + (h * 4 + b) * 256 + r * 16 : OOB;
    asm volatile("s_cmp_eq_u32 %5, 0\n\ts_cbranch_scc0 1f\n\tbuffer_load_dwordx2 %0, %1, %4, 0 offen sc1\n\t"
                 "buffer_store_dwordx4 %2, %3, %4, 0 offen sc1\n\ts_nop 1\n1:"
                 : "+v"(ep) : "v"(OFF_EP), "v"(sent), "v"(co), "s"(rws), "s"(w) : "memory", "scc");
  }
  K_PIECE(1);
  WAIT_VM(8);
  asm volatile("" : "+v"(ep));
  if (w == 0) {
    tag = ((unsigned long long)(unsigned)__builtin_amdgcn_readfirstlane(ep.y) << 32 | (unsigned)__builtin_amdgcn_readfirstlane(ep.x)) + 1ull;
    const v2u tg = {(unsigned)tag, (unsigned)(tag >> 32)};
    asm volatile("buffer_store_dwordx2 %0, %1, %2, 0 offen sc1\n\ts_nop 0" ::"v"(tg), "v"(l == 0 ? OFF_FLAG + cu * 8 : OOB), "s"(rws) : "memory");
  }
  K_READ(0); K_PIECE(2);
  WAIT_VM(8); K_READ(1); K_PIECE(3); SVC(1);
  WAIT_VM(8); K_READ(2); V_PIECE(0, 0); SVC(2);
  WAIT_VM(8); K_READ(3); V_PIECE(1, 1); SVC(3);
  WAIT_VM(8); V_READ(0); V_PIECE(2, 0); SVC(4);
  WAIT_VM(8); V_READ(1); V_PIECE(3, 1); SVC(5);
  STAMP(1);
#pragma unroll 1
  for (int pass = 0; pass < 2; ++pass) {
    if (pass == 0) {
      for (int spin = 0; *(volatile LAS unsigned*)(L + L_QF) == 0u; ++spin) {
        if (spin > (SPIN_CAP << 4)) { dead = true; break; }
        __builtin_amdgcn_s_sleep(1);
      }
#pragma unroll
      for (int s = 0; s < 4; ++s) qf[s] = *(LAS v4u*)(L + L_Q + s * 64 + g * 16);
      asm volatile("" ::: "memory");
    } else {
      hold(now(), T_O_Q);
      int lo = l; asm volatile("" : "+v"(lo));
      const int qo = OFF_Q + SZ_Q + (h * 16 + b) * 64 + (lo >> 4) * 16;
      const int off[4] = {qo, qo + 256, qo + 512, qo + 768};
      gather<4>(qf, rws, off, dead);
    }
    STAMP2(pass, 4, 5);
    attn_math(pass, L, kf, vf, qf, rws, b, h, w, l, st);
  }
}

__device__ __forceinline__ f4 red_sum(LAS unsigned char* L, int row, int l) {
  const int hf = l >> 5, cg = l & 7;
  f4 s = *(LAS f4*)(L + L_RED + (hf * 4) * RED_W + row * 144 + cg * 16);
#pragma unroll
  for (int i = 1; i < 4; ++i) s += *(LAS f4*)(L + L_RED + (hf * 4 + i) * RED_W + row * 144 + cg * 16);
  return f4{x32_sum(s[0]), x32_sum(s[1]), x32_sum(s[2]), x32_sum(s[3])};
}
__device__ __forceinline__ f2 red_stat(LAS unsigned char* L, int row, int l) {
  const int hf = l >> 5;
  f2 s = *(LAS f2*)(L + L_RED + (hf * 4) * RED_W + row * 144 + 128);
#pragma unroll
  for (int i = 1; i < 4; ++i) s += *(LAS f2*)(L + L_RED + (hf * 4 + i) * RED_W + row * 144 + 128);
  return f2{x32_sum(s[0]), x32_sum(s[1])};
}

__device__ __forceinline__ f4 red_sum_gu(LAS unsigned char* L, int row, int l) {
  const int m = l >> 5, cg = l & 7;
  f4 s = *(LAS f4*)(L + L_RED + m * RED_W + row * 144 + cg * 16);
#pragma unroll
  for (int i = 1; i < 4; ++i) s += *(LAS f4*)(L + L_RED + (2 * i + m) * RED_W + row * 144 + cg * 16);
  return s;
}
struct WArgs { const u16 *x, *wq, *wo, *wg, *wu, *wd; u16* out; };

template <int PASS>
__device__ __forceinline__ void q_phase(LAS unsigned char* L, const v4u (&xf)[3], const v4u (&wqf)[6], f4 csq, rsrc_t rws, int tq, int w, int l, LAS unsigned* st) {
  const int g = l >> 4, c = l & 15;
  const f4 z = {0.f, 0.f, 0.f, 0.f};
  const v4u ones = {0x3F803F80u, 0x3F803F80u, 0x3F803F80u, 0x3F803F80u};
  f4 d0 = z, d1 = z, ds = z;
  float ss = 0.f;
#pragma unroll
  for (int i = 0; i < 3; ++i) {
    d0 = mfma(wqf[2 * i], xf[i], d0); d1 = mfma(wqf[2 * i + 1], xf[i], d1); ds = mfma(ones, xf[i], ds);
    ss = dot2(xf[i].x, xf[i].x, ss); ss = dot2(xf[i].y, xf[i].y, ss); ss = dot2(xf[i].z, xf[i].z, ss); ss = dot2(xf[i].w, xf[i].w, ss);
  }
  ss = xrow_sum(ss);
  LAS unsigned char* R = L + L_RED + w * RED_W + c * 144;
  if (c < 4) {
    *(LAS f4*)(R + g * 16) = d0; *(LAS f4*)(R + 64 + g * 16) = d1;
    if (g == 0) *(LAS f2*)(R + 128) = f2{ds[0], ss};
  }
  __syncthreads();
  if (PASS == 1) STAMP(22);
  if (w == 0) {
    const int b = (l >> 3) & 3, cg = l & 7;
    const f4 p = red_sum(L, b, l);
    const f2 s = red_stat(L, b, l);
    const float mu = s[0] * (1.0f / D), var = fmaxf(s[1] * (1.0f / D) - mu * mu, 0.f), rs = __builtin_amdgcn_rsqf(var + 1e-5f);
    if (l < 32) {
      const v2u o = {pack_bf16((p[0] - mu * csq[0]) * rs, (p[1] - mu * csq[1]) * rs), pack_bf16((p[2] - mu * csq[2]) * rs, (p[3] - mu * csq[3]) * rs)};
      __builtin_amdgcn_raw_buffer_store_b64(o, rws, OFF_Q + PASS * SZ_Q + tq * 256 + b * 64 + cg * 8, 0, AUX_SC1);
    }
  }
  __syncthreads();
}

__device__ __forceinline__ void q_second(LAS unsigned char* L, const v4u (&wqf)[6], f4 csq, rsrc_t rws, int tq, int w, int l, bool& dead,
                                         LAS unsigned* st) {
  const int g = l >> 4, c = l & 15;
  v4u xf[3];
  int off[3];
#pragma unroll
  for (int i = 0; i < 3; ++i) {
    const int s = w + 8 * i;
    off[i] = (c < 4 && s < 20) ? OFF_X2 + ((2 * s + (g >> 1)) * 4 + c) * 32 + (g & 1) * 16 : OOB;
  }
  gather<3>(xf, rws, off, dead);
  STAMP(6);
  q_phase<1>(L, xf, wqf, csq, rws, tq, w, l, st);
  STAMP(7);
}

__device__ __forceinline__ void weight_role(LAS unsigned char* L, const WArgs& a, rsrc_t rws, int cu, int xc, int u, int w, int l,
                                            LAS unsigned* st) {
  const int g = l >> 4, r = l & 15, c = r;
  const f4 z = {0.f, 0.f, 0.f, 0.f};
  const v4u ones = {0x3F803F80u, 0x3F803F80u, 0x3F803F80u, 0x3F803F80u};
  v4u sel1, sel2;
  {
    const int j = r - 8 * (g & 1);
    const unsigned one = (j & 1) ? 0x3F800000u : 0x00003F80u;
    const bool in = (unsigned)j < 8u;
    v4u s = {(in && (j >> 1) == 0) ? one : 0u, (in && (j >> 1) == 1) ? one : 0u, (in && (j >> 1) == 2) ? one : 0u, (in && (j >> 1) == 3) ? one : 0u};
    const v4u zz = {0u, 0u, 0u, 0u};
    sel1 = g < 2 ? s : zz; sel2 = g < 2 ? zz : s;
  }
  const int tq = xc * 16 + u;
  const bool has_wo = u < 10, has_gu = u < (xc < 3 ? 8 : 6), has_dn = u >= 10 && u < 15;
  const int wo_id = xc * 10 + u, kq = wo_id / 20, nt32 = wo_id % 20;
  const int t32 = xc < 3 ? xc * 8 + u : 24 + (xc - 3) * 6 + u;
  const int td = xc * 5 + (u - 10);
  const int rho16 = 8 * (r >> 2) + (r & 3);
  const int rho32 = rho16 + 4 * (g >> 1);

  bool dead = false;
  const unsigned long long tstart = __builtin_amdgcn_s_memrealtime();
  v2u ep = {0u, 0u};
  if (w == 0) {
    ep = __builtin_amdgcn_raw_buffer_load_b64(rws, OFF_EP, 0, AUX_SC1);
    const int hf = l >> 5, k = l & 31;
    clear16(rws, k < 16 ? OFF_Q + hf * SZ_Q + tq * 256 + k * 16 : has_wo ? OFF_X1 + hf * SZ_X1 + (kq * 40 + 2 * nt32) * 128 + (k - 16) * 16 : OOB);
    clear16(rws, (k < 16 && has_gu) ? OFF_H + hf * SZ_H + t32 * 256 + k * 16 : (k < 8 && hf == 0 && has_dn) ? OFF_X2 + td * 128 + k * 16 : OOB);
  }
  v4u wqf[6];
  unsigned long long tag = 0;
  f4 csq;
  {
    v4u xf[3];
    const rsrc_t rx = mkrsrc(a.x, NB * D * 2), rq = mkrsrc(a.wq, D * HD * 2);
#pragma unroll
    for (int i = 0; i < 3; ++i) {
      const int s = w + 8 * i;
      xf[i] = ld128<0>(rx, (c < 4 && s < 20) ? c * 1280 + s * 64 + g * 16 : OOB, 0);
    }
#pragma unroll
    for (int i = 0; i < 3; ++i) {
      const int s = w + 8 * i;
      const int vo = s < 20 ? (32 * s + rho16) * 8192 + tq * 64 + g * 16 : OOB;
      wqf[2 * i] = ld128<AUX_NT>(rq, vo, 0); wqf[2 * i + 1] = ld128<AUX_NT>(rq, vo, 4 * 8192);
    }
    __builtin_amdgcn_sched_barrier(0);
    if (w == 0) {
      asm volatile("s_waitcnt vmcnt(9)" : "+v"(ep)::"memory");
      tag = ((unsigned long long)(unsigned)__builtin_amdgcn_readfirstlane(ep.y) << 32 | (unsigned)__builtin_amdgcn_readfirstlane(ep.x)) + 1ull;
      if (l == 0) __builtin_amdgcn_raw_buffer_store_b64(v2u{(unsigned)tag, (unsigned)(tag >> 32)}, rws, OFF_FLAG + cu * 8, 0, AUX_SC1);
    }
#pragma unroll
    for (int i = 0; i < 3; ++i) xpose32(wqf[2 * i], wqf[2 * i + 1], sel1, sel2);
    f4 c0 = z, c1 = z;
#pragma unroll
    for (int i = 0; i < 3; ++i) { c0 = mfma(wqf[2 * i], ones, c0); c1 = mfma(wqf[2 * i + 1], ones, c1); }
    LAS unsigned char* R4 = L + L_RED + w * RED_W + 4 * 144;
    if (c == 0) { *(LAS f4*)(R4 + g * 16) = c0; *(LAS f4*)(R4 + 64 + g * 16) = c1; }
    __syncthreads();
    csq = red_sum(L, 4, l);
    q_phase<0>(L, xf, wqf, csq, rws, tq, w, l, st);
  }
  STAMP(1);
  const int fb = (l >> 3) & 3, fcg = l & 7;
  LAS unsigned char* R = L + L_RED + w * RED_W + c * 144;
#define HOLD() do { while ((long long)(__builtin_amdgcn_s_memrealtime() - tstart) < HOLD_T) __builtin_amdgcn_s_sleep(16); } while (0)

  if (has_wo) {
    v4u wof[8], guf[10];
    const int gm = w & 1, gq = w >> 1;
    {
      const rsrc_t ro = mkrsrc(a.wo, D * HD * 2), rgu = mkrsrc(gm ? a.wu : a.wg, D * FF * 2);
      const int vo = ((8 * kq + w) * 128 + rho16) * 1280 + nt32 * 64 + g * 16;
#pragma unroll
      for (int s = 0; s < 4; ++s) { wof[2 * s] = ld128<AUX_NT>(ro, vo, s * 32 * 1280); wof[2 * s + 1] = ld128<AUX_NT>(ro, vo, (s * 32 + 4) * 1280); }
      const int vg = has_gu ? (160 * gq + rho16) * 3456 + t32 * 64 + g * 16 : OOB;
#pragma unroll
      for (int i = 0; i < 5; ++i) { guf[2 * i] = ld128<AUX_NT>(rgu, vg, i * 32 * 3456); guf[2 * i + 1] = ld128<AUX_NT>(rgu, vg, (i * 32 + 4) * 3456); }
      __builtin_amdgcn_sched_barrier(0);
    }
#pragma unroll
    for (int s = 0; s < 4; ++s) xpose32(wof[2 * s], wof[2 * s + 1], sel1, sel2);
#pragma unroll
    for (int i = 0; i < 5; ++i) xpose32(guf[2 * i], guf[2 * i + 1], sel1, sel2);
    f4 csg;
    {
      f4 c0 = z, c1 = z;
#pragma unroll
      for (int i = 0; i < 5; ++i) { c0 = mfma(guf[2 * i], ones, c0); c1 = mfma(guf[2 * i + 1], ones, c1); }
      LAS unsigned char* R4 = L + L_RED + w * RED_W + 4 * 144;
      if (c == 0) { *(LAS f4*)(R4 + g * 16) = c0; *(LAS f4*)(R4 + 64 + g * 16) = c1; }
      if (w == 0) flags_wait(rws, tag, l, dead);
      __syncthreads();
      csg = red_sum_gu(L, 4, l);
    }
    STAMP(2);
    HOLD();
#pragma unroll 1
    for (int pass = 0; pass < 2; ++pass) {
      {
        v4u of[4];
        int lo = l; asm volatile("" : "+v"(lo));
        const int o0 = (lo & 15) < 4 ? OFF_O + pass * SZ_O + ((8 * kq + w) * 4 + (lo & 15)) * 256 + (lo >> 4) * 16 : OOB;
        const int off[4] = {o0, o0 + 64, o0 + 128, o0 + 192};
        v2u res = {0u, 0u};
        if (w == 0 && kq == 0 && l < 32) {
          if (pass == 0) res = *(const v2u*)(a.x + fb * D + nt32 * 32 + fcg * 4);
          else res = __builtin_amdgcn_raw_buffer_load_b64(rws, OFF_X2 + ((2 * nt32 + (fcg >> 2)) * 4 + fb) * 32 + (fcg & 3) * 8, 0, AUX_SC1);
        }
        gather<4>(of, rws, off, dead);
        STAMP2(pass, 3, 9);
        f4 d0 = z, d1 = z;
#pragma unroll
        for (int s = 0; s < 4; ++s) { d0 = mfma(wof[2 * s], of[s], d0); d1 = mfma(wof[2 * s + 1], of[s], d1); }
        if (c < 4) { *(LAS f4*)(R + g * 16) = d0; *(LAS f4*)(R + 64 + g * 16) = d1; }
        __syncthreads();
        STAMP2(pass, 16, 24);
        if (w == 0) {
          f4 p = red_sum(L, fb, l);
          p[0] += bflo(res.x); p[1] += bfhi(res.x); p[2] += bflo(res.y); p[3] += bfhi(res.y);
          if (l < 32) {
            const v2u o = {pack_bf16(p[0], p[1]), pack_bf16(p[2], p[3])};
            __builtin_amdgcn_raw_buffer_store_b64(o, rws, OFF_X1 + pass * SZ_X1 + ((kq * 40 + 2 * nt32 + (fcg >> 2)) * 4 + fb) * 32 + (fcg & 3) * 8, 0, AUX_SC1);
          }
          STAMP2(pass, 17, 25);
        }
        __syncthreads();
      }
      if (has_gu) {
        v4u x1[5];
        int xo[5];
        int lo = l; asm volatile("" : "+v"(lo));
#pragma unroll
        for (int i = 0; i < 5; ++i)
          xo[i] = OFF_X1 + pass * SZ_X1 + ((((lo & 15) >> 2) * 40 + 2 * (5 * gq + i) + (lo >> 5)) * 4 + (lo & 3)) * 32 + ((lo >> 4) & 1) * 16;
        SETTLE();
        gather<5>(x1, rws, xo, dead);
        STAMP2(pass, 4, 10);
        f4 d0 = z, d1 = z, ds = z;
#pragma unroll
        for (int i = 0; i < 5; ++i) { d0 = mfma(guf[2 * i], x1[i], d0); d1 = mfma(guf[2 * i + 1], x1[i], d1); }
        if (gm == 0) {
#pragma unroll
          for (int i = 0; i < 5; ++i) ds = mfma(ones, x1[i], ds);
        }
#pragma unroll
        for (int i = 0; i < 4; ++i) {
          d0[i] += dpp<ROR4>(d0[i]); d0[i] += dpp<ROR8>(d0[i]);
          d1[i] += dpp<ROR4>(d1[i]); d1[i] += dpp<ROR8>(d1[i]);
        }
        float sx = ds[0]; sx += dpp<ROR4>(sx); sx += dpp<ROR8>(sx);
        if (c < 4) {
          *(LAS f4*)(R + g * 16) = d0; *(LAS f4*)(R + 64 + g * 16) = d1;
          if (g == 0) *(LAS float*)(R + 128) = sx;
        }
        __syncthreads();
        STAMP2(pass, 18, 26);
        if (w == 0) {
          const f4 p = red_sum_gu(L, fb, l);
          float sm = 0.f;
#pragma unroll
          for (int i = 0; i < 4; ++i) sm += *(LAS float*)(L + L_RED + 2 * i * RED_W + fb * 144 + 128);
          const float mu = sm * (1.0f / D);
          float hv[4];
#pragma unroll
          for (int i = 0; i < 4; ++i) {
            const float v = p[i] - mu * csg[i];
            auto sw = __builtin_amdgcn_permlane32_swap(__float_as_uint(v), __float_as_uint(v), false, false);
            hv[i] = fmaxf(__uint_as_float(sw[0]), 0.f) * __uint_as_float(sw[1]);
          }
          if (l < 32) {
            const v2u o = {pack_bf16(hv[0], hv[1]), pack_bf16(hv[2], hv[3])};
            __builtin_amdgcn_raw_buffer_store_b64(o, rws, OFF_H + pass * SZ_H + ((2 * t32 + (fcg >> 2)) * 4 + fb) * 32 + (fcg & 3) * 8, 0, AUX_SC1);
          }
          STAMP2(pass, 19, 27);
        }
        __syncthreads();
      }
      if (pass == 0) { hold(now(), has_gu ? T_GU_X2 : T_WO_X2); q_second(L, wqf, csq, rws, tq, w, l, dead, st); hold(now(), T_Q_O); }
    }
  } else {
    v4u wdf[7];
#define T_DN_LOAD 900
    hold(tstart, T_DN_LOAD);
    {
      const rsrc_t rd = mkrsrc(a.wd, D * FF * 2);
#pragma unroll
      for (int i = 0; i < 7; ++i) {
        const int s = w + 8 * i;
        wdf[i] = ld128<AUX_NT>(rd, (has_dn && s < 54) ? (32 * s + rho32) * 1280 + td * 32 + (g & 1) * 16 : OOB, 0);
      }
      __builtin_amdgcn_sched_barrier(0);
    }
#pragma unroll
    for (int i = 0; i < 7; ++i) wdf[i] = xpose16(wdf[i], sel1, sel2);
    if (w == 0) flags_wait(rws, tag, l, dead);
    __syncthreads();
    STAMP(2);
    HOLD();
#pragma unroll 1
    for (int pass = 0; pass < 2; ++pass) {
      if (has_dn) {
        v4u x1[3];
        int xoff[3];
        int lo = l; asm volatile("" : "+v"(lo));
#pragma unroll
        for (int i = 0; i < 3; ++i) {
          const int s = w + 8 * i;
          xoff[i] = s < 20 ? OFF_X1 + pass * SZ_X1 + ((((lo & 15) >> 2) * 40 + 2 * s + (lo >> 5)) * 4 + (lo & 3)) * 32 + ((lo >> 4) & 1) * 16 : OOB;
        }
        gather<3>(x1, rws, xoff, dead);
        const unsigned long long tx1 = now();
        STAMP2(pass, 4, 10);
        f4 gram = z, ds = z;
#pragma unroll
        for (int i = 0; i < 3; ++i) { gram = mfma(x1[i], x1[i], gram); ds = mfma(ones, x1[i], ds); }
        float sq = (c & 3) == 0 ? gram[0] : (c & 3) == 1 ? gram[1] : (c & 3) == 2 ? gram[2] : gram[3];
        sq += dpp<ROR4>(sq); sq += dpp<ROR8>(sq); sq = xrow_sum(sq);
        float sx = ds[0]; sx += dpp<ROR4>(sx); sx += dpp<ROR8>(sx);
        if (c < 4 && g == 0) *(LAS f2*)(R + 128) = f2{sx, sq};
        __syncthreads();
        float rs2 = 0.f; f4 res = z;
        if (w == 0) {
          v2u rp[4];
#pragma unroll
          for (int k = 0; k < 4; ++k)
            rp[k] = __builtin_amdgcn_raw_buffer_load_b64(rws, OFF_X1 + pass * SZ_X1 + ((k * 40 + td) * 4 + fb) * 32 + (fcg & 3) * 8, 0, AUX_SC1);
          const f2 s = red_stat(L, fb, l);
          const float mu = s[0] * (1.0f / D), var = fmaxf(s[1] * (1.0f / D) - mu * mu, 0.f);
          rs2 = __builtin_amdgcn_rcpf(var + 1e-5f);
#pragma unroll
          for (int k = 0; k < 4; ++k) { res[0] += bflo(rp[k].x); res[1] += bfhi(rp[k].x); res[2] += bflo(rp[k].y); res[3] += bfhi(rp[k].y); }
        }
        v4u hf[7];
        int hoff[7];
#pragma unroll
        for (int i = 0; i < 7; ++i) {
          const int s = w + 8 * i;
          hoff[i] = ((lo & 15) < 4 && s < 54) ? OFF_H + pass * SZ_H + ((2 * s + (lo >> 5)) * 4 + (lo & 15)) * 32 + ((lo >> 4) & 1) * 16 : OOB;
        }
        hold(tx1, T_X1_H);
        gather<7>(hf, rws, hoff, dead);
        STAMP2(pass, 5, 11);
        f4 d = z;
#pragma unroll
        for (int i = 0; i < 7; ++i) d = mfma(wdf[i], hf[i], d);
        if (c < 4) { *(LAS f4*)(R + g * 16) = d; *(LAS f4*)(R + 64 + g * 16) = z; }
        __syncthreads();
        STAMP2(pass, 20, 28);
        if (w == 0) {
          const f4 p = red_sum(L, fb, l);
          if (l < 32 && fcg < 4) {
            f4 o;
#pragma unroll
            for (int i = 0; i < 4; ++i) o[i] = res[i] + rs2 * p[i];
            if (dead) o = f4{__builtin_nanf(""), __builtin_nanf(""), __builtin_nanf(""), __builtin_nanf("")};
            const v2u ob = {pack_bf16(o[0], o[1]), pack_bf16(o[2], o[3])};
            if (pass == 0) __builtin_amdgcn_raw_buffer_store_b64(ob, rws, OFF_X2 + (td * 4 + fb) * 32 + fcg * 8, 0, AUX_SC1);
            else *(v2u*)(a.out + fb * D + td * 16 + fcg * 4) = ob;
          }
          STAMP2(pass, 21, 29);
          if (pass == 1 && td == 0 && l == 0) __builtin_amdgcn_raw_buffer_store_b64(v2u{(unsigned)tag, (unsigned)(tag >> 32)}, rws, OFF_EP, 0, AUX_SC1);
        }
        __syncthreads();
      }
      if (pass == 0) { if (has_dn) SETTLE(); q_second(L, wqf, csq, rws, tq, w, l, dead, st); hold(now(), T_Q_X3); }
    }
  }
}

__global__ __launch_bounds__(512, 2) void mega_kernel(const u16* __restrict__ x, const u16* __restrict__ K, const u16* __restrict__ V,
                                                      const u16* __restrict__ wq, const u16* __restrict__ wo, const u16* __restrict__ wg,
                                                      const u16* __restrict__ wu, const u16* __restrict__ wd, u16* __restrict__ out,
                                                      unsigned char* __restrict__ ws) {
  asm volatile("" ::"s"(x), "s"(K), "s"(V), "s"(wq), "s"(wo), "s"(wg), "s"(wu), "s"(wd), "s"(out), "s"(ws));
  __shared__ __attribute__((aligned(16))) unsigned char lds[L_END];
  LAS unsigned char* L = (LAS unsigned char*)lds;
  const int cu = blockIdx.x, xc = cu & 7, y = cu >> 3;
  const int l = threadIdx.x & 63, w = __builtin_amdgcn_readfirstlane(threadIdx.x >> 6);
  const rsrc_t rws = mkrsrc(ws, WS_USED);
  LAS unsigned* st = (LAS unsigned*)(L + L_ST) + w * 32;
  if (y < 16) attn_role(L, K, V, rws, cu, xc, y, w, l, st);
  else {
    WArgs a{x, wq, wo, wg, wu, wd, out};
    weight_role(L, a, rws, cu, xc, y - 16, w, l, st);
  }
}
}

extern "C" void kernel_launch(void* const* d_in, const int* in_sizes, int n_in, void* d_out, int out_size, void* d_ws, size_t ws_size,
                              hipStream_t stream) {
  (void)in_sizes; (void)n_in; (void)out_size; (void)ws_size;
  mega_kernel<<<256, 512, 0, stream>>>((const u16*)d_in[0], (const u16*)d_in[1], (const u16*)d_in[2], (const u16*)d_in[3],
                                       (const u16*)d_in[4], (const u16*)d_in[6], (const u16*)d_in[7], (const u16*)d_in[8],
                                       (u16*)d_out, (unsigned char*)d_ws);
}
```

```cpp
#include <hip/hip_runtime.h>
#include <stdint.h>

#define MK_STAMPS 0
#define HOLD_T 1280

namespace {
typedef unsigned short u16;
typedef unsigned v4u __attribute__((ext_vector_type(4)));
typedef unsigned v2u __attribute__((ext_vector_type(2)));
typedef float f4 __attribute__((ext_vector_type(4)));
typedef float f2 __attribute__((ext_vector_type(2)));
typedef __bf16 bf8 __attribute__((ext_vector_type(8)));
typedef __bf16 bf2 __attribute__((ext_vector_type(2)));
typedef short s4 __attribute__((ext_vector_type(4)));
typedef __amdgpu_buffer_rsrc_t rsrc_t;
#define LAS __attribute__((address_space(3)))

constexpr int NB = 4, D = 640, NH = 32, DH = 128, SK = 4096, WIN = 1024, FF = 1728, HD = 4096;
constexpr unsigned SENT = 0xFFFFFFFFu;
constexpr int OOB = 0x40000000;
constexpr int SZ_Q = 32 * 4 * 256, SZ_O = 32 * 4 * 256, SZ_X1 = 4 * 40 * 128, SZ_H = 108 * 128, SZ_X2 = 40 * 128;
constexpr int OFF_Q = 0, OFF_O = OFF_Q + 2 * SZ_Q, OFF_X1 = OFF_O + 2 * SZ_O, OFF_H = OFF_X1 + 2 * SZ_X1,
              OFF_X2 = OFF_H + 2 * SZ_H, OFF_PROBE = OFF_X2 + SZ_X2, DATA_END = OFF_PROBE + 8 * 128,
              OFF_FLAG = 2 * ((DATA_END + 8191) & ~8191), OFF_EP = OFF_FLAG + 2048, OFF_NEAR = OFF_EP + 128, WS_USED = OFF_NEAR + 128;
__device__ __forceinline__ int dup(int off, int copy) { return off + (off & ~8191) + (copy << 13); }
constexpr int OFF_STAMP = 1 << 20;
constexpr int SPIN_CAP = 1 << 17;
constexpr int L_V = 0;
constexpr int L_P = 131072;
constexpr int L_OW = L_P + 2048;
constexpr int L_ML = L_OW + 4096;
constexpr int L_Q = L_ML + 64;
constexpr int L_QF = L_Q + 256;
constexpr int L_ST = L_QF + 16;
constexpr int L_END = L_ST + (MK_STAMPS ? 1024 : 0);
constexpr int L_RED = 0;
constexpr int RED_W = 5 * 36 * 4;
constexpr int L_NEAR = 6144;

constexpr int AUX_NT = 2, AUX_SC1 = 16;

__device__ __forceinline__ f4 mfma(v4u a, v4u b, f4 c) {
  return __builtin_amdgcn_mfma_f32_16x16x32_bf16(__builtin_bit_cast(bf8, a), __builtin_bit_cast(bf8, b), c, 0, 0, 0);
}
__device__ __forceinline__ unsigned hi16pack(float hi, float lo) {
  return __builtin_amdgcn_perm(__builtin_bit_cast(unsigned, hi), __builtin_bit_cast(unsigned, lo), 0x07060302u);
}
__device__ __forceinline__ unsigned pack_bf16(float lo, float hi) {
  bf2 r = {(__bf16)lo, (__bf16)hi};
  unsigned u = __builtin_bit_cast(unsigned, r);
  return u == SENT ? 0x7FFF7FFFu : u;
}
__device__ __forceinline__ float bflo(unsigned u) { return __builtin_bit_cast(float, u << 16); }
__device__ __forceinline__ float bfhi(unsigned u) { return __builtin_bit_cast(float, u & 0xFFFF0000u); }
__device__ __forceinline__ float dot2(unsigned a, unsigned b, float c) {
  return __builtin_amdgcn_fdot2_f32_bf16(__builtin_bit_cast(bf2, a), __builtin_bit_cast(bf2, b), c, false);
}
template <int CTRL> __device__ __forceinline__ float dpp(float x) {
  return __builtin_bit_cast(float, __builtin_amdgcn_mov_dpp(__builtin_bit_cast(int, x), CTRL, 0xf, 0xf, true));
}
constexpr int XOR1 = 0xB1, XOR2 = 0x4E, ROR4 = 0x124, ROR8 = 0x128, SHL4 = 0x104;
__device__ __forceinline__ float xrow_sum(float x) {
  auto s = __builtin_amdgcn_permlane16_swap(__float_as_uint(x), __float_as_uint(x), false, false);
  x = __uint_as_float(s[0]) + __uint_as_float(s[1]);
  auto t = __builtin_amdgcn_permlane32_swap(__float_as_uint(x), __float_as_uint(x), false, false);
  return __uint_as_float(t[0]) + __uint_as_float(t[1]);
}
__device__ __forceinline__ float xrow_max(float x) {
  auto s = __builtin_amdgcn_permlane16_swap(__float_as_uint(x), __float_as_uint(x), false, false);
  x = fmaxf(__uint_as_float(s[0]), __uint_as_float(s[1]));
  auto t = __builtin_amdgcn_permlane32_swap(__float_as_uint(x), __float_as_uint(x), false, false);
  return fmaxf(__uint_as_float(t[0]), __uint_as_float(t[1]));
}
__device__ __forceinline__ float x32_sum(float x) {
  auto t = __builtin_amdgcn_permlane32_swap(__float_as_uint(x), __float_as_uint(x), false, false);
  return __uint_as_float(t[0]) + __uint_as_float(t[1]);
}
__device__ __forceinline__ rsrc_t mkrsrc(const void* p, unsigned bytes) {
  return __builtin_amdgcn_make_buffer_rsrc((void*)p, 0, bytes, 0x00020000);
}
template <int AUX> __device__ __forceinline__ v4u ld128(rsrc_t rs, int voff, int soff) {
  return __builtin_amdgcn_raw_buffer_load_b128(rs, voff, soff, AUX);
}
__device__ __forceinline__ unsigned umax3(unsigned a, unsigned b, unsigned c) { return max(max(a, b), c); }
__device__ __forceinline__ unsigned vmax4(v4u x, unsigned m) { return umax3(x.x, x.y, umax3(x.z, x.w, m)); }

#define NPROBE 5
#define DLY 11
__device__ __forceinline__ unsigned long long now() { return __builtin_amdgcn_s_memrealtime(); }
__device__ __forceinline__ void hold(unsigned long long t0, int ticks) {
  if (ticks > 0) while ((long long)(now() - t0) < ticks) __builtin_amdgcn_s_sleep(1);
}
#define GSLEEP 0
#define T_NEAR 1150
#define T_X1_H 75
#define T_WO_X2 265
#define T_GU_X2 140
#define T_Q_O 215
#define T_Q_X3 330
#define T_O_Q 450
#define SETTLE() __builtin_amdgcn_s_sleep(DLY)
__device__ __forceinline__ int xcc_id() { return __builtin_amdgcn_s_getreg(20 | (3 << 11)) & 7; }
__device__ __forceinline__ void near_probe(rsrc_t rws, int slot, unsigned tag, int l) {
  unsigned best0 = ~0u, best1 = ~0u;
#pragma unroll 1
  for (int i = 0; i < NPROBE; ++i) {
    unsigned long long t0, t1, t2;
    const int o0 = l == 0 ? dup(OFF_PROBE + slot * 128, 0) : OOB, o1 = l == 0 ? dup(OFF_PROBE + slot * 128, 1) : OOB;
    asm volatile("s_waitcnt vmcnt(0)\n\ts_memtime %0\n\ts_waitcnt lgkmcnt(0)\n\tbuffer_store_dword %3, %4, %6, 0 offen sc1\n\ts_waitcnt vmcnt(0)\n\t"
                 "s_memtime %1\n\ts_waitcnt lgkmcnt(0)\n\tbuffer_store_dword %3, %5, %6, 0 offen sc1\n\ts_waitcnt vmcnt(0)\n\ts_memtime %2\n\ts_waitcnt lgkmcnt(0)"
                 : "=&s"(t0), "=&s"(t1), "=&s"(t2) : "v"(i), "v"(o0), "v"(o1), "s"(rws) : "memory");
    best0 = min(best0, (unsigned)(t1 - t0)); best1 = min(best1, (unsigned)(t2 - t1));
  }
  if (l == 0) __builtin_amdgcn_raw_buffer_store_b32((tag << 1) | (best1 < best0 ? 1u : 0u), rws, OFF_NEAR + xcc_id() * 4, 0, AUX_SC1);
}
__device__ __forceinline__ int near_fetch(rsrc_t rws, unsigned tag) {
  const unsigned v = __builtin_amdgcn_readfirstlane(__builtin_amdgcn_raw_buffer_load_b32(rws, OFF_NEAR + xcc_id() * 4, 0, AUX_SC1));
  return (v >> 1) == (tag & 0x7FFFFFFFu) ? (int)(v & 1u) : 0;
}
template <int N> __device__ __forceinline__ void gather(v4u (&x)[N], rsrc_t rs, const int (&off_)[N], bool& dead, int nc) {
  int off[N];
#pragma unroll
  for (int i = 0; i < N; ++i) off[i] = dup(off_[i], nc);
  for (int spin = 0;; ++spin) {
    asm volatile("" ::: "memory");
#pragma unroll
    for (int i = 0; i < N; ++i) x[i] = ld128<AUX_SC1>(rs, off[i], 0);
    unsigned m = 0u;
#pragma unroll
    for (int i = 0; i < N; ++i) m = vmax4(x[i], m);
    if (!__any(m == SENT)) break;
    if (dead || spin > SPIN_CAP) { dead = true; break; }
    if (GSLEEP) __builtin_amdgcn_s_sleep(GSLEEP);
  }
}
__device__ __forceinline__ unsigned long long epoch_load(rsrc_t rws) {
  const v2u e = __builtin_amdgcn_raw_buffer_load_b64(rws, OFF_EP, 0, AUX_SC1);
  return ((unsigned long long)(unsigned)__builtin_amdgcn_readfirstlane(e.y) << 32 | (unsigned)__builtin_amdgcn_readfirstlane(e.x)) + 1ull;
}
__device__ __forceinline__ void clear16(rsrc_t rws, int off) {
  __builtin_amdgcn_raw_buffer_store_b128(v4u{SENT, SENT, SENT, SENT}, rws, dup(off, 0), 0, AUX_SC1);
  __builtin_amdgcn_raw_buffer_store_b128(v4u{SENT, SENT, SENT, SENT}, rws, dup(off, 1), 0, AUX_SC1);
}
__device__ __forceinline__ void flag_post(rsrc_t rws, int cu, unsigned long long tag, int l) {
  asm volatile("s_waitcnt vmcnt(0)" ::: "memory");
  if (l == 0) __builtin_amdgcn_raw_buffer_store_b64(v2u{(unsigned)tag, (unsigned)(tag >> 32)}, rws, OFF_FLAG + cu * 8, 0, AUX_SC1);
}
__device__ __forceinline__ void flags_wait(rsrc_t rws, unsigned long long tag, int l, bool& dead) {
  const unsigned lo = (unsigned)tag, hi = (unsigned)(tag >> 32);
  for (int spin = 0;; ++spin) {
    asm volatile("" ::: "memory");
    const v4u a = ld128<AUX_SC1>(rws, OFF_FLAG + l * 32, 0), b = ld128<AUX_SC1>(rws, OFF_FLAG + l * 32 + 16, 0);
    const bool ok = (a.x == lo) & (a.y == hi) & (a.z == lo) & (a.w == hi) & (b.x == lo) & (b.y == hi) & (b.z == lo) & (b.w == hi);
    if (__all(ok)) break;
    if (dead || spin > SPIN_CAP) { dead = true; break; }
    __builtin_amdgcn_s_sleep(4);
  }
}
__device__ __forceinline__ v4u xpose16(v4u raw, v4u sel1, v4u sel2) {
  const f4 z = {0.f, 0.f, 0.f, 0.f};
  const f4 d1 = mfma(raw, sel1, z), d2 = mfma(raw, sel2, z);
  return v4u{hi16pack(d1[1], d1[0]), hi16pack(d1[3], d1[2]), hi16pack(d2[1], d2[0]), hi16pack(d2[3], d2[2])};
}
__device__ __forceinline__ void xpose32(v4u& a, v4u& b, v4u sel1, v4u sel2) {
  const f4 z = {0.f, 0.f, 0.f, 0.f};
  const f4 a1 = mfma(a, sel1, z), b1 = mfma(b, sel1, z), a2 = mfma(a, sel2, z), b2 = mfma(b, sel2, z);
  a = v4u{hi16pack(a1[1], a1[0]), hi16pack(a1[3], a1[2]), hi16pack(b1[1], b1[0]), hi16pack(b1[3], b1[2])};
  b = v4u{hi16pack(a2[1], a2[0]), hi16pack(a2[3], a2[2]), hi16pack(b2[1], b2[0]), hi16pack(b2[3], b2[2])};
}

#define STAMP(i) do { } while (0)
#define STAMP2(p, a, b) do { } while (0)

#define ASM_LD(dst, voff, rs, soff, MODS) \
  asm volatile("buffer_load_dwordx4 %0, %1, %2, %3 offen " MODS : "=v"(dst) : "v"(voff), "s"(rs), "s"(soff) : "memory")
#define ASM_DMA(voff, rs, soff, ldsa) \
  asm volatile("s_mov_b32 m0, %3\n\ts_nop 0\n\tbuffer_load_dwordx4 %0, %1, %2 offen nt lds" ::"v"(voff), "s"(rs), "s"(soff), "s"(ldsa) : "memory")
#define WAIT_VM(n) asm volatile("s_waitcnt vmcnt(" #n ")" ::: "memory")
#define PIN8(a, o) asm volatile("" : "+v"(a[o]), "+v"(a[o + 1]), "+v"(a[o + 2]), "+v"(a[o + 3]), "+v"(a[o + 4]), "+v"(a[o + 5]), "+v"(a[o + 6]), "+v"(a[o + 7]))

__device__ __forceinline__ v2u tr_read(LAS unsigned char* p) {
  s4 t = __builtin_amdgcn_ds_read_tr16_b64_v4i16((LAS s4*)p);
  return __builtin_bit_cast(v2u, t);
}

__device__ __forceinline__ void attn_math(const int PASS, LAS unsigned char* L, const v4u (&kf)[32], const v4u (&vf)[16], const v4u (&qf)[4],
                                          rsrc_t rws, int b, int h, int w, int l_, LAS unsigned* st) {
  int l = l_; asm volatile("" : "+v"(l));
  const int g = l >> 4;
  const f4 z = {0.f, 0.f, 0.f, 0.f};
  constexpr float CS = 0.08838834764831845f * 1.4426950408889634f;
  float s0 = 0.f, s1 = 0.f;
#pragma unroll
  for (int t = 0; t < 8; t += 2) {
    f4 d0 = z, d1 = z;
#pragma unroll
    for (int s = 0; s < 4; ++s) { d0 = mfma(qf[s], kf[t * 4 + s], d0); d1 = mfma(qf[s], kf[t * 4 + 4 + s], d1); }
    if (t < 4) { s0 = (g == t) ? d0[0] : s0; s0 = (g == t + 1) ? d1[0] : s0; }
    else { s1 = (g == t - 4) ? d0[0] : s1; s1 = (g == t - 3) ? d1[0] : s1; }
  }
  STAMP2(PASS, 16, 8);
  float m = fmaxf(s0, s1);
  m = fmaxf(m, dpp<XOR1>(m)); m = fmaxf(m, dpp<XOR2>(m)); m = fmaxf(m, dpp<ROR4>(m)); m = fmaxf(m, dpp<ROR8>(m));
  m = xrow_max(m);
  const __bf16 p0 = (__bf16)__builtin_amdgcn_exp2f((s0 - m) * CS), p1 = (__bf16)__builtin_amdgcn_exp2f((s1 - m) * CS);
  LAS __bf16* pw = (LAS __bf16*)(L + L_P + w * 256);
  pw[l] = p0; pw[64 + l] = p1;
  v4u pf[4];
#pragma unroll
  for (int s = 0; s < 4; ++s) pf[s] = *(LAS v4u*)(L + L_P + w * 256 + s * 64 + g * 16);
  STAMP2(PASS, 17, 9);
  float ls = (float)p0 + (float)p1;
  ls += dpp<XOR1>(ls); ls += dpp<XOR2>(ls); ls += dpp<ROR4>(ls); ls += dpp<ROR8>(ls);
  ls = xrow_sum(ls);
  float oa0 = 0.f, oa1 = 0.f;
#pragma unroll
  for (int n = 0; n < 8; n += 2) {
    f4 d0 = mfma(pf[0], vf[n], z), d1 = mfma(pf[0], vf[n + 1], z);
    d0 = mfma(pf[1], vf[8 + n], d0); d1 = mfma(pf[1], vf[9 + n], d1);
    if (n < 4) { oa0 = (g == n) ? d0[0] : oa0; oa0 = (g == n + 1) ? d1[0] : oa0; }
    else { oa1 = (g == n - 4) ? d0[0] : oa1; oa1 = (g == n - 3) ? d1[0] : oa1; }
  }
  const int q4 = (l >> 2) & 3;
  const int trb = L_V + w * 16384 + (8 * g + q4) * 256 + ((q4 | ((g & 1) << 2)) << 5) + (l & 3) * 8;
  float ob0 = 0.f, ob1 = 0.f;
#pragma unroll
  for (int hb = 0; hb < 2; ++hb) {
    if (hb == 0) WAIT_VM(8); else WAIT_VM(0);
    if (hb == 1) STAMP2(PASS, 2, 6);
    v2u t0[4], t1[4];
    float pc0 = 0.f, pc1 = 0.f;
#define TR_LOAD(n) do { LAS unsigned char* a_ = L + (trb ^ ((n) << 5)) + hb * 8192; t0[(n) & 3] = tr_read(a_); t1[(n) & 3] = tr_read(a_ + 1024); } while (0)
    TR_LOAD(0); TR_LOAD(1);
#pragma unroll
    for (int n = 0; n < 8; n += 2) {
      if (n < 6) { TR_LOAD(n + 2); TR_LOAD(n + 3); }
      __builtin_amdgcn_sched_barrier(0);
      const f4 d0 = mfma(pf[2 + hb], v4u{t0[n & 3].x, t0[n & 3].y, t1[n & 3].x, t1[n & 3].y}, z);
      const f4 d1 = mfma(pf[2 + hb], v4u{t0[(n + 1) & 3].x, t0[(n + 1) & 3].y, t1[(n + 1) & 3].x, t1[(n + 1) & 3].y}, z);
      if (n < 4) { pc0 = (g == n) ? d0[0] : pc0; pc0 = (g == n + 1) ? d1[0] : pc0; }
      else { pc1 = (g == n - 4) ? d0[0] : pc1; pc1 = (g == n - 3) ? d1[0] : pc1; }
    }
    ob0 += pc0; ob1 += pc1;
  }
  LAS float* ow = (LAS float*)(L + L_OW);
  ow[w * 128 + l] = oa0 + ob0; ow[w * 128 + 64 + l] = oa1 + ob1;
  if (l == 0) *(LAS f2*)(L + L_ML + w * 8) = f2{m, ls};
  STAMP2(PASS, 18, 10);
  __syncthreads();
  STAMP2(PASS, 19, 11);
  if (w == 0) {
    f4 ml[4];
    f2 ov[8];
#pragma unroll
    for (int i = 0; i < 4; ++i) ml[i] = *(LAS f4*)(L + L_ML + i * 16);
#pragma unroll
    for (int i = 0; i < 8; ++i) ov[i] = *(LAS f2*)(L + L_OW + i * 512 + l * 8);
    float M = fmaxf(fmaxf(fmaxf(ml[0][0], ml[0][2]), fmaxf(ml[1][0], ml[1][2])), fmaxf(fmaxf(ml[2][0], ml[2][2]), fmaxf(ml[3][0], ml[3][2])));
    float Ls = 0.f, o0 = 0.f, o1 = 0.f;
#pragma unroll
    for (int i = 0; i < 8; ++i) {
      const float f = __builtin_amdgcn_exp2f((ml[i >> 1][(i & 1) * 2] - M) * CS);
      Ls = fmaf(f, ml[i >> 1][(i & 1) * 2 + 1], Ls); o0 = fmaf(f, ov[i].x, o0); o1 = fmaf(f, ov[i].y, o1);
    }
    const float inv = __builtin_amdgcn_rcpf(Ls);
    const unsigned ob = pack_bf16(o0 * inv, o1 * inv);
    const int oo = OFF_O + PASS * SZ_O + (h * 4 + b) * 256 + l * 4;
    __builtin_amdgcn_raw_buffer_store_b32(ob, rws, dup(oo, 0), 0, AUX_SC1);
    __builtin_amdgcn_raw_buffer_store_b32(ob, rws, dup(oo, 1), 0, AUX_SC1);
  }
  STAMP2(PASS, 3, 7);
}

__device__ __forceinline__ void attn_role(LAS unsigned char* L, const u16* K, const u16* V, rsrc_t rws, int cu, int xc, int y, int w, int l,
                                          LAS unsigned* st) {
  const int h = 4 * xc + (y >> 2), b = y & 3, g = l >> 4, r = l & 15;
  const size_t kv = ((size_t)(b * NH + h) * SK + (SK - WIN) + 128 * w) * DH;
  const rsrc_t rK = mkrsrc(K + kv, 32768), rV = mkrsrc(V + kv, 32768);
  constexpr int PSTR = 8192;
  bool dead = false;
#define SVC_AT 3
#define SVC(n) do { if (SVC_AT == (n) && w == 0) { \
      flags_wait(rws, tag, l, dead); \
      v4u q1[1]; const int off[1] = {l < 16 ? OFF_Q + (h * 16 + b) * 64 + (l >> 2) * 256 + (l & 3) * 16 : OOB}; \
      gather<1>(q1, rws, off, dead, 0); \
      if (l < 16) *(LAS v4u*)(L + L_Q + l * 16) = q1[0]; \
      asm volatile("s_waitcnt lgkmcnt(0)" ::: "memory"); \
      if (l == 0) *(volatile LAS unsigned*)(L + L_QF) = 1u; } } while (0)
  v4u kf[32], vf[16], qf[4];
  const int ldsV = L_V + w * 16384;
  const int ks0 = g * 256 + ((r ^ g) << 4);
#define KX(i) (((i) & 3) << 6)
  const int vs0 = g * 256 + (((r >> 1) ^ g) << 5) + (l & 1) * 16, vs1 = g * 256 + (((r >> 1) ^ (g | 4)) << 5) + (l & 1) * 16;
#define K_PIECE(n) _Pragma("unroll") for (int i = 0; i < 8; ++i) \
    ASM_DMA(ks0 ^ KX(i), rK, (n) * PSTR + i * 1024, ldsV + ((n) & 1) * 8192 + i * 1024)
#define V_PIECE(n, buf) _Pragma("unroll") for (int i = 0; i < 8; ++i) \
    ASM_DMA((i & 2) ? vs1 : vs0, rV, (n) * PSTR + i * 1024, ldsV + (buf) * 8192 + i * 1024)
#define K_READ(n) do { _Pragma("unroll") for (int t = 0; t < 2; ++t) _Pragma("unroll") for (int s = 0; s < 4; ++s) \
      kf[((n) * 2 + t) * 4 + s] = *(LAS v4u*)(L + ldsV + ((n) & 1) * 8192 + (16 * t + r) * 256 + (((4 * s + g) ^ r) << 4)); \
    asm volatile("s_waitcnt lgkmcnt(0)" ::: "memory"); PIN8(kf, (n) * 8); } while (0)
  const int trb = ldsV + (8 * g + ((l >> 2) & 3)) * 256 + ((((l >> 2) & 3) | ((g & 1) << 2)) << 5) + (l & 3) * 8;
#define V_READ(n) do { _Pragma("unroll") for (int c8 = 0; c8 < 8; ++c8) { \
      LAS unsigned char* a = L + (trb ^ (c8 << 5)) + (n) * 8192; \
      const v2u t0 = tr_read(a), t1 = tr_read(a + 1024); \
      vf[(n) * 8 + c8] = v4u{t0.x, t0.y, t1.x, t1.y}; } \
    asm volatile("s_waitcnt lgkmcnt(0)" ::: "memory"); PIN8(vf, (n) * 8); } while (0)
  K_PIECE(0);
  if (threadIdx.x == 0) *(volatile LAS unsigned*)(L + L_QF) = 0u;
  asm volatile("s_waitcnt lgkmcnt(0)\n\ts_barrier" ::: "memory");
  v2u ep = {0u, 0u};
  unsigned long long tag = 0;
  {
    const v4u sent = {SENT, SENT, SENT, SENT};
    const int co = dup(OFF_O + ((l >> 4) & 1) * SZ_O + (h * 4 + b) * 256 + r * 16, l >> 5);
    asm volatile("s_cmp_eq_u32 %5, 0\n\ts_cbranch_scc0 1f\n\tbuffer_load_dwordx2 %0, %1, %4, 0 offen sc1\n\t"
                 "buffer_store_dwordx4 %2, %3, %4, 0 offen sc1\n\ts_nop 1\n1:"
                 : "+v"(ep) : "v"(OFF_EP), "v"(sent), "v"(co), "s"(rws), "s"(w) : "memory", "scc");
  }
  K_PIECE(1);
  WAIT_VM(8);
  asm volatile("" : "+v"(ep));
  if (w == 0) {
    tag = ((unsigned long long)(unsigned)__builtin_amdgcn_readfirstlane(ep.y) << 32 | (unsigned)__builtin_amdgcn_readfirstlane(ep.x)) + 1ull;
    const v2u tg = {(unsigned)tag, (unsigned)(tag >> 32)};
    asm volatile("buffer_store_dwordx2 %0, %1, %2, 0 offen sc1\n\ts_nop 0" ::"v"(tg), "v"(l == 0 ? OFF_FLAG + cu * 8 : OOB), "s"(rws) : "memory");
  }
  K_READ(0); K_PIECE(2);
  WAIT_VM(8); K_READ(1); K_PIECE(3); SVC(1);
  WAIT_VM(8); K_READ(2); V_PIECE(0, 0); SVC(2);
  WAIT_VM(8); K_READ(3); V_PIECE(1, 1); SVC(3);
  WAIT_VM(8); V_READ(0); V_PIECE(2, 0); SVC(4);
  WAIT_VM(8); V_READ(1); V_PIECE(3, 1); SVC(5);
  STAMP(1);
#pragma unroll 1
  for (int pass = 0; pass < 2; ++pass) {
    if (pass == 0) {
      for (int spin = 0; *(volatile LAS unsigned*)(L + L_QF) == 0u; ++spin) {
        if (spin > (SPIN_CAP << 4)) { dead = true; break; }
        __builtin_amdgcn_s_sleep(1);
      }
#pragma unroll
      for (int s = 0; s < 4; ++s) qf[s] = *(LAS v4u*)(L + L_Q + s * 64 + g * 16);
      asm volatile("" ::: "memory");
    } else {
      const unsigned long long tr = now();
      if (w == 0) { const int n_ = near_fetch(rws, (unsigned)tag); if (l == 0) *(volatile LAS unsigned*)(L + L_QF + 4) = n_; }
      __syncthreads();
      const int nc = __builtin_amdgcn_readfirstlane(*(volatile LAS unsigned*)(L + L_QF + 4));
      hold(tr, T_O_Q);
      int lo = l; asm volatile("" : "+v"(lo));
      const int qo = OFF_Q + SZ_Q + (h * 16 + b) * 64 + (lo >> 4) * 16;
      const int off[4] = {qo, qo + 256, qo + 512, qo + 768};
      gather<4>(qf, rws, off, dead, nc);
    }
    STAMP2(pass, 4, 5);
    attn_math(pass, L, kf, vf, qf, rws, b, h, w, l, st);
  }
}

__device__ __forceinline__ f4 red_sum(LAS unsigned char* L, int row, int l) {
  const int hf = l >> 5, cg = l & 7;
  f4 s = *(LAS f4*)(L + L_RED + (hf * 4) * RED_W + row * 144 + cg * 16);
#pragma unroll
  for (int i = 1; i < 4; ++i) s += *(LAS f4*)(L + L_RED + (hf * 4 + i) * RED_W + row * 144 + cg * 16);
  return f4{x32_sum(s[0]), x32_sum(s[1]), x32_sum(s[2]), x32_sum(s[3])};
}
__device__ __forceinline__ f2 red_stat(LAS unsigned char* L, int row, int l) {
  const int hf = l >> 5;
  f2 s = *(LAS f2*)(L + L_RED + (hf * 4) * RED_W + row * 144 + 128);
#pragma unroll
  for (int i = 1; i < 4; ++i) s += *(LAS f2*)(L + L_RED + (hf * 4 + i) * RED_W + row * 144 + 128);
  return f2{x32_sum(s[0]), x32_sum(s[1])};
}

__device__ __forceinline__ f4 red_sum_gu(LAS unsigned char* L, int row, int l) {
  const int m = l >> 5, cg = l & 7;
  f4 s = *(LAS f4*)(L + L_RED + m * RED_W + row * 144 + cg * 16);
#pragma unroll
  for (int i = 1; i < 4; ++i) s += *(LAS f4*)(L + L_RED + (2 * i + m) * RED_W + row * 144 + cg * 16);
  return s;
}
struct WArgs { const u16 *x, *wq, *wo, *wg, *wu, *wd; u16* out; };

template <int PASS>
__device__ __forceinline__ void q_phase(LAS unsigned char* L, const v4u (&xf)[3], const v4u (&wqf)[6], f4 csq, rsrc_t rws, int tq, int w, int l, LAS unsigned* st) {
  const int g = l >> 4, c = l & 15;
  const f4 z = {0.f, 0.f, 0.f, 0.f};
  const v4u ones = {0x3F803F80u, 0x3F803F80u, 0x3F803F80u, 0x3F803F80u};
  f4 d0 = z, d1 = z, ds = z;
  float ss = 0.f;
#pragma unroll
  for (int i = 0; i < 3; ++i) {
    d0 = mfma(wqf[2 * i], xf[i], d0); d1 = mfma(wqf[2 * i + 1], xf[i], d1); ds = mfma(ones, xf[i], ds);
    ss = dot2(xf[i].x, xf[i].x, ss); ss = dot2(xf[i].y, xf[i].y, ss); ss = dot2(xf[i].z, xf[i].z, ss); ss = dot2(xf[i].w, xf[i].w, ss);
  }
  ss = xrow_sum(ss);
  LAS unsigned char* R = L + L_RED + w * RED_W + c * 144;
  if (c < 4) {
    *(LAS f4*)(R + g * 16) = d0; *(LAS f4*)(R + 64 + g * 16) = d1;
    if (g == 0) *(LAS f2*)(R + 128) = f2{ds[0], ss};
  }
  __syncthreads();
  if (PASS == 1) STAMP(22);
  if (w == 0) {
    const int b = (l >> 3) & 3, cg = l & 7;
    const f4 p = red_sum(L, b, l);
    const f2 s = red_stat(L, b, l);
    const float mu = s[0] * (1.0f / D), var = fmaxf(s[1] * (1.0f / D) - mu * mu, 0.f), rs = __builtin_amdgcn_rsqf(var + 1e-5f);
    {
      const v2u o = {pack_bf16((p[0] - mu * csq[0]) * rs, (p[1] - mu * csq[1]) * rs), pack_bf16((p[2] - mu * csq[2]) * rs, (p[3] - mu * csq[3]) * rs)};
      __builtin_amdgcn_raw_buffer_store_b64(o, rws, dup(OFF_Q + PASS * SZ_Q + tq * 256 + b * 64 + cg * 8, l >> 5), 0, AUX_SC1);
    }
  }
  __syncthreads();
}

__device__ __forceinline__ void q_second(LAS unsigned char* L, const v4u (&wqf)[6], f4 csq, rsrc_t rws, int tq, int w, int l, bool& dead, int nc,
                                         LAS unsigned* st) {
  const int g = l >> 4, c = l & 15;
  v4u xf[3];
  int off[3];
#pragma unroll
  for (int i = 0; i < 3; ++i) {
    const int s = w + 8 * i;
    off[i] = (c < 4 && s < 20) ? OFF_X2 + ((2 * s + (g >> 1)) * 4 + c) * 32 + (g & 1) * 16 : OOB;
  }
  gather<3>(xf, rws, off, dead, nc);
  STAMP(6);
  q_phase<1>(L, xf, wqf, csq, rws, tq, w, l, st);
  STAMP(7);
}

__device__ __forceinline__ void weight_role(LAS unsigned char* L, const WArgs& a, rsrc_t rws, int cu, int xc, int u, int w, int l,
                                            LAS unsigned* st) {
  const int g = l >> 4, r = l & 15, c = r;
  const f4 z = {0.f, 0.f, 0.f, 0.f};
  const v4u ones = {0x3F803F80u, 0x3F803F80u, 0x3F803F80u, 0x3F803F80u};
  v4u sel1, sel2;
  {
    const int j = r - 8 * (g & 1);
    const unsigned one = (j & 1) ? 0x3F800000u : 0x00003F80u;
    const bool in = (unsigned)j < 8u;
    v4u s = {(in && (j >> 1) == 0) ? one : 0u, (in && (j >> 1) == 1) ? one : 0u, (in && (j >> 1) == 2) ? one : 0u, (in && (j >> 1) == 3) ? one : 0u};
    const v4u zz = {0u, 0u, 0u, 0u};
    sel1 = g < 2 ? s : zz; sel2 = g < 2 ? zz : s;
  }
  const int tq = xc * 16 + u;
  const bool has_wo = u < 10, has_gu = u < (xc < 3 ? 8 : 6), has_dn = u >= 10 && u < 15;
  const int wo_id = xc * 10 + u, kq = wo_id / 20, nt32 = wo_id % 20;
  const int t32 = xc < 3 ? xc * 8 + u : 24 + (xc - 3) * 6 + u;
  const int td = xc * 5 + (u - 10);
  const int rho16 = 8 * (r >> 2) + (r & 3);
  const int rho32 = rho16 + 4 * (g >> 1);

  bool dead = false;
  const unsigned long long tstart = __builtin_amdgcn_s_memrealtime();
  v2u ep = {0u, 0u};
  if (w == 0) {
    ep = __builtin_amdgcn_raw_buffer_load_b64(rws, OFF_EP, 0, AUX_SC1);
    const int hf = l >> 5, k = l & 31;
    clear16(rws, k < 16 ? OFF_Q + hf * SZ_Q + tq * 256 + k * 16 : has_wo ? OFF_X1 + hf * SZ_X1 + (kq * 40 + 2 * nt32) * 128 + (k - 16) * 16 : OOB);
    clear16(rws, (k < 16 && has_gu) ? OFF_H + hf * SZ_H + t32 * 256 + k * 16 : (k < 8 && hf == 0 && has_dn) ? OFF_X2 + td * 128 + k * 16 : OOB);
  }
  v4u wqf[6];
  unsigned long long tag = 0;
  f4 csq;
  {
    v4u xf[3];
    const rsrc_t rx = mkrsrc(a.x, NB * D * 2), rq = mkrsrc(a.wq, D * HD * 2);
#pragma unroll
    for (int i = 0; i < 3; ++i) {
      const int s = w + 8 * i;
      xf[i] = ld128<0>(rx, (c < 4 && s < 20) ? c * 1280 + s * 64 + g * 16 : OOB, 0);
    }
#pragma unroll
    for (int i = 0; i < 3; ++i) {
      const int s = w + 8 * i;
      const int vo = s < 20 ? (32 * s + rho16) * 8192 + tq * 64 + g * 16 : OOB;
      wqf[2 * i] = ld128<AUX_NT>(rq, vo, 0); wqf[2 * i + 1] = ld128<AUX_NT>(rq, vo, 4 * 8192);
    }
    __builtin_amdgcn_sched_barrier(0);
    if (w == 0) {
      asm volatile("s_waitcnt vmcnt(9)" : "+v"(ep)::"memory");
      tag = ((unsigned long long)(unsigned)__builtin_amdgcn_readfirstlane(ep.y) << 32 | (unsigned)__builtin_amdgcn_readfirstlane(ep.x)) + 1ull;
      if (l == 0) __builtin_amdgcn_raw_buffer_store_b64(v2u{(unsigned)tag, (unsigned)(tag >> 32)}, rws, OFF_FLAG + cu * 8, 0, AUX_SC1);
    }
#pragma unroll
    for (int i = 0; i < 3; ++i) xpose32(wqf[2 * i], wqf[2 * i + 1], sel1, sel2);
    f4 c0 = z, c1 = z;
#pragma unroll
    for (int i = 0; i < 3; ++i) { c0 = mfma(wqf[2 * i], ones, c0); c1 = mfma(wqf[2 * i + 1], ones, c1); }
    LAS unsigned char* R4 = L + L_RED + w * RED_W + 4 * 144;
    if (c == 0) { *(LAS f4*)(R4 + g * 16) = c0; *(LAS f4*)(R4 + 64 + g * 16) = c1; }
    __syncthreads();
    csq = red_sum(L, 4, l);
    q_phase<0>(L, xf, wqf, csq, rws, tq, w, l, st);
  }
  STAMP(1);
  const int fb = (l >> 3) & 3, fcg = l & 7;
  LAS unsigned char* R = L + L_RED + w * RED_W + c * 144;
#define HOLD() do { while ((long long)(__builtin_amdgcn_s_memrealtime() - tstart) < HOLD_T) __builtin_amdgcn_s_sleep(16); } while (0)

  if (has_wo) {
    v4u wof[8], guf[10];
    const int gm = w & 1, gq = w >> 1;
    {
      const rsrc_t ro = mkrsrc(a.wo, D * HD * 2), rgu = mkrsrc(gm ? a.wu : a.wg, D * FF * 2);
      const int vo = ((8 * kq + w) * 128 + rho16) * 1280 + nt32 * 64 + g * 16;
#pragma unroll
      for (int s = 0; s < 4; ++s) { wof[2 * s] = ld128<AUX_NT>(ro, vo, s * 32 * 1280); wof[2 * s + 1] = ld128<AUX_NT>(ro, vo, (s * 32 + 4) * 1280); }
      const int vg = has_gu ? (160 * gq + rho16) * 3456 + t32 * 64 + g * 16 : OOB;
#pragma unroll
      for (int i = 0; i < 5; ++i) { guf[2 * i] = ld128<AUX_NT>(rgu, vg, i * 32 * 3456); guf[2 * i + 1] = ld128<AUX_NT>(rgu, vg, (i * 32 + 4) * 3456); }
      __builtin_amdgcn_sched_barrier(0);
    }
#pragma unroll
    for (int s = 0; s < 4; ++s) xpose32(wof[2 * s], wof[2 * s + 1], sel1, sel2);
#pragma unroll
    for (int i = 0; i < 5; ++i) xpose32(guf[2 * i], guf[2 * i + 1], sel1, sel2);
    f4 csg;
    {
      f4 c0 = z, c1 = z;
#pragma unroll
      for (int i = 0; i < 5; ++i) { c0 = mfma(guf[2 * i], ones, c0); c1 = mfma(guf[2 * i + 1], ones, c1); }
      LAS unsigned char* R4 = L + L_RED + w * RED_W + 4 * 144;
      if (c == 0) { *(LAS f4*)(R4 + g * 16) = c0; *(LAS f4*)(R4 + 64 + g * 16) = c1; }
      if (w == 0) flags_wait(rws, tag, l, dead);
      __syncthreads();
      csg = red_sum_gu(L, 4, l);
    }
    STAMP(2);
    hold(tstart, T_NEAR);
    if (w == 0) { const int n_ = near_fetch(rws, (unsigned)tag); if (l == 0) *(volatile LAS unsigned*)(L + L_NEAR) = n_; }
    __syncthreads();
    const int nc = __builtin_amdgcn_readfirstlane(*(volatile LAS unsigned*)(L + L_NEAR));
    HOLD();
#pragma unroll 1
    for (int pass = 0; pass < 2; ++pass) {
      {
        v4u of[4];
        int lo = l; asm volatile("" : "+v"(lo));
        const int o0 = (lo & 15) < 4 ? OFF_O + pass * SZ_O + ((8 * kq + w) * 4 + (lo & 15)) * 256 + (lo >> 4) * 16 : OOB;
        const int off[4] = {o0, o0 + 64, o0 + 128, o0 + 192};
        v2u res = {0u, 0u};
        if (w == 0 && kq == 0) {
          if (pass == 0) res = *(const v2u*)(a.x + fb * D + nt32 * 32 + fcg * 4);
          else res = __builtin_amdgcn_raw_buffer_load_b64(rws, dup(OFF_X2 + ((2 * nt32 + (fcg >> 2)) * 4 + fb) * 32 + (fcg & 3) * 8, nc), 0, AUX_SC1);
        }
        gather<4>(of, rws, off, dead, nc);
        STAMP2(pass, 3, 9);
        f4 d0 = z, d1 = z;
#pragma unroll
        for (int s = 0; s < 4; ++s) { d0 = mfma(wof[2 * s], of[s], d0); d1 = mfma(wof[2 * s + 1], of[s], d1); }
        if (c < 4) { *(LAS f4*)(R + g * 16) = d0; *(LAS f4*)(R + 64 + g * 16) = d1; }
        __syncthreads();
        STAMP2(pass, 16, 24);
        if (w == 0) {
          f4 p = red_sum(L, fb, l);
          p[0] += bflo(res.x); p[1] += bfhi(res.x); p[2] += bflo(res.y); p[3] += bfhi(res.y);
          {
            const v2u o = {pack_bf16(p[0], p[1]), pack_bf16(p[2], p[3])};
            __builtin_amdgcn_raw_buffer_store_b64(o, rws, dup(OFF_X1 + pass * SZ_X1 + ((kq * 40 + 2 * nt32 + (fcg >> 2)) * 4 + fb) * 32 + (fcg & 3) * 8, l >> 5), 0, AUX_SC1);
          }
          STAMP2(pass, 17, 25);
        }
        __syncthreads();
      }
      if (has_gu) {
        v4u x1[5];
        int xo[5];
        int lo = l; asm volatile("" : "+v"(lo));
#pragma unroll
        for (int i = 0; i < 5; ++i)
          xo[i] = OFF_X1 + pass * SZ_X1 + ((((lo & 15) >> 2) * 40 + 2 * (5 * gq + i) + (lo >> 5)) * 4 + (lo & 3)) * 32 + ((lo >> 4) & 1) * 16;
        SETTLE();
        gather<5>(x1, rws, xo, dead, nc);
        STAMP2(pass, 4, 10);
        f4 d0 = z, d1 = z, ds = z;
#pragma unroll
        for (int i = 0; i < 5; ++i) { d0 = mfma(guf[2 * i], x1[i], d0); d1 = mfma(guf[2 * i + 1], x1[i], d1); }
        if (gm == 0) {
#pragma unroll
          for (int i = 0; i < 5; ++i) ds = mfma(ones, x1[i], ds);
        }
#pragma unroll
        for (int i = 0; i < 4; ++i) {
          d0[i] += dpp<ROR4>(d0[i]); d0[i] += dpp<ROR8>(d0[i]);
          d1[i] += dpp<ROR4>(d1[i]); d1[i] += dpp<ROR8>(d1[i]);
        }
        float sx = ds[0]; sx += dpp<ROR4>(sx); sx += dpp<ROR8>(sx);
        if (c < 4) {
          *(LAS f4*)(R + g * 16) = d0; *(LAS f4*)(R + 64 + g * 16) = d1;
          if (g == 0) *(LAS float*)(R + 128) = sx;
        }
        __syncthreads();
        STAMP2(pass, 18, 26);
        if (w == 0) {
          const f4 p = red_sum_gu(L, fb, l);
          float sm = 0.f;
#pragma unroll
          for (int i = 0; i < 4; ++i) sm += *(LAS float*)(L + L_RED + 2 * i * RED_W + fb * 144 + 128);
          const float mu = sm * (1.0f / D);
          float hv[4];
#pragma unroll
          for (int i = 0; i < 4; ++i) {
            const float v = p[i] - mu * csg[i];
            auto sw = __builtin_amdgcn_permlane32_swap(__float_as_uint(v), __float_as_uint(v), false, false);
            hv[i] = fmaxf(__uint_as_float(sw[0]), 0.f) * __uint_as_float(sw[1]);
          }
          {
            const v2u o = {pack_bf16(hv[0], hv[1]), pack_bf16(hv[2], hv[3])};
            __builtin_amdgcn_raw_buffer_store_b64(o, rws, dup(OFF_H + pass * SZ_H + ((2 * t32 + (fcg >> 2)) * 4 + fb) * 32 + (fcg & 3) * 8, l >> 5), 0, AUX_SC1);
          }
          STAMP2(pass, 19, 27);
        }
        __syncthreads();
      }
      if (pass == 0) { hold(now(), has_gu ? T_GU_X2 : T_WO_X2); q_second(L, wqf, csq, rws, tq, w, l, dead, nc, st); hold(now(), T_Q_O); }
    }
  } else {
    v4u wdf[7];
    if (!has_dn && w == 0) near_probe(rws, xc, (unsigned)tag, l);
#define T_DN_LOAD 900
    hold(tstart, T_DN_LOAD);
    {
      const rsrc_t rd = mkrsrc(a.wd, D * FF * 2);
#pragma unroll
      for (int i = 0; i < 7; ++i) {
        const int s = w + 8 * i;
        wdf[i] = ld128<AUX_NT>(rd, (has_dn && s < 54) ? (32 * s + rho32) * 1280 + td * 32 + (g & 1) * 16 : OOB, 0);
      }
      __builtin_amdgcn_sched_barrier(0);
    }
#pragma unroll
    for (int i = 0; i < 7; ++i) wdf[i] = xpose16(wdf[i], sel1, sel2);
    if (w == 0) flags_wait(rws, tag, l, dead);
    __syncthreads();
    STAMP(2);
    hold(tstart, T_NEAR);
    if (w == 0) { const int n_ = near_fetch(rws, (unsigned)tag); if (l == 0) *(volatile LAS unsigned*)(L + L_NEAR) = n_; }
    __syncthreads();
    const int nc = __builtin_amdgcn_readfirstlane(*(volatile LAS unsigned*)(L + L_NEAR));
    HOLD();
#pragma unroll 1
    for (int pass = 0; pass < 2; ++pass) {
      if (has_dn) {
        v4u x1[3];
        int xoff[3];
        int lo = l; asm volatile("" : "+v"(lo));
#pragma unroll
        for (int i = 0; i < 3; ++i) {
          const int s = w + 8 * i;
          xoff[i] = s < 20 ? OFF_X1 + pass * SZ_X1 + ((((lo & 15) >> 2) * 40 + 2 * s + (lo >> 5)) * 4 + (lo & 3)) * 32 + ((lo >> 4) & 1) * 16 : OOB;
        }
        gather<3>(x1, rws, xoff, dead, nc);
        const unsigned long long tx1 = now();
        STAMP2(pass, 4, 10);
        f4 gram = z, ds = z;
#pragma unroll
        for (int i = 0; i < 3; ++i) { gram = mfma(x1[i], x1[i], gram); ds = mfma(ones, x1[i], ds); }
        float sq = (c & 3) == 0 ? gram[0] : (c & 3) == 1 ? gram[1] : (c & 3) == 2 ? gram[2] : gram[3];
        sq += dpp<ROR4>(sq); sq += dpp<ROR8>(sq); sq = xrow_sum(sq);
        float sx = ds[0]; sx += dpp<ROR4>(sx); sx += dpp<ROR8>(sx);
        if (c < 4 && g == 0) *(LAS f2*)(R + 128) = f2{sx, sq};
        __syncthreads();
        float rs2 = 0.f; f4 res = z;
        if (w == 0) {
          v2u rp[4];
#pragma unroll
          for (int k = 0; k < 4; ++k)
            rp[k] = __builtin_amdgcn_raw_buffer_load_b64(rws, dup(OFF_X1 + pass * SZ_X1 + ((k * 40 + td) * 4 + fb) * 32 + (fcg & 3) * 8, nc), 0, AUX_SC1);
          const f2 s = red_stat(L, fb, l);
          const float mu = s[0] * (1.0f / D), var = fmaxf(s[1] * (1.0f / D) - mu * mu, 0.f);
          rs2 = __builtin_amdgcn_rcpf(var + 1e-5f);
#pragma unroll
          for (int k = 0; k < 4; ++k) { res[0] += bflo(rp[k].x); res[1] += bfhi(rp[k].x); res[2] += bflo(rp[k].y); res[3] += bfhi(rp[k].y); }
        }
        v4u hf[7];
        int hoff[7];
#pragma unroll
        for (int i = 0; i < 7; ++i) {
          const int s = w + 8 * i;
          hoff[i] = ((lo & 15) < 4 && s < 54) ? OFF_H + pass * SZ_H + ((2 * s + (lo >> 5)) * 4 + (lo & 15)) * 32 + ((lo >> 4) & 1) * 16 : OOB;
        }
        hold(tx1, T_X1_H);
        gather<7>(hf, rws, hoff, dead, nc);
        STAMP2(pass, 5, 11);
        f4 d = z;
#pragma unroll
        for (int i = 0; i < 7; ++i) d = mfma(wdf[i], hf[i], d);
        if (c < 4) { *(LAS f4*)(R + g * 16) = d; *(LAS f4*)(R + 64 + g * 16) = z; }
        __syncthreads();
        STAMP2(pass, 20, 28);
        if (w == 0) {
          const f4 p = red_sum(L, fb, l);
          if (fcg < 4) {
            f4 o;
#pragma unroll
            for (int i = 0; i < 4; ++i) o[i] = res[i] + rs2 * p[i];
            if (dead) o = f4{__builtin_nanf(""), __builtin_nanf(""), __builtin_nanf(""), __builtin_nanf("")};
            const v2u ob = {pack_bf16(o[0], o[1]), pack_bf16(o[2], o[3])};
            if (pass == 0) __builtin_amdgcn_raw_buffer_store_b64(ob, rws, dup(OFF_X2 + (td * 4 + fb) * 32 + fcg * 8, l >> 5), 0, AUX_SC1);
            else if (l < 32) *(v2u*)(a.out + fb * D + td * 16 + fcg * 4) = ob;
          }
          STAMP2(pass, 21, 29);
          if (pass == 1 && td == 0 && l == 0) __builtin_amdgcn_raw_buffer_store_b64(v2u{(unsigned)tag, (unsigned)(tag >> 32)}, rws, OFF_EP, 0, AUX_SC1);
        }
        __syncthreads();
      }
      if (pass == 0) { if (has_dn) SETTLE(); q_second(L, wqf, csq, rws, tq, w, l, dead, nc, st); hold(now(), T_Q_X3); }
    }
  }
}

__global__ __launch_bounds__(512, 2) void mega_kernel(const u16* __restrict__ x, const u16* __restrict__ K, const u16* __restrict__ V,
                                                      const u16* __restrict__ wq, const u16* __restrict__ wo, const u16* __restrict__ wg,
                                                      const u16* __restrict__ wu, const u16* __restrict__ wd, u16* __restrict__ out,
                                                      unsigned char* __restrict__ ws) {
  asm volatile("" ::"s"(x), "s"(K), "s"(V), "s"(wq), "s"(wo), "s"(wg), "s"(wu), "s"(wd), "s"(out), "s"(ws));
  __shared__ __attribute__((aligned(16))) unsigned char lds[L_END];
  LAS unsigned char* L = (LAS unsigned char*)lds;
  const int cu = blockIdx.x, xc = cu & 7, y = cu >> 3;
  const int l = threadIdx.x & 63, w = __builtin_amdgcn_readfirstlane(threadIdx.x >> 6);
  const rsrc_t rws = mkrsrc(ws, WS_USED);
  LAS unsigned* st = (LAS unsigned*)(L + L_ST) + w * 32;
  if (y < 16) attn_role(L, K, V, rws, cu, xc, y, w, l, st);
  else {
    WArgs a{x, wq, wo, wg, wu, wd, out};
    weight_role(L, a, rws, cu, xc, y - 16, w, l, st);
  }
}
}

extern "C" void kernel_launch(void* const* d_in, const int* in_sizes, int n_in, void* d_out, int out_size, void* d_ws, size_t ws_size,
                              hipStream_t stream) {
  (void)in_sizes; (void)n_in; (void)out_size; (void)ws_size;
  mega_kernel<<<256, 512, 0, stream>>>((const u16*)d_in[0], (const u16*)d_in[1], (const u16*)d_in[2], (const u16*)d_in[3],
                                       (const u16*)d_in[4], (const u16*)d_in[6], (const u16*)d_in[7], (const u16*)d_in[8],
                                       (u16*)d_out, (unsigned char*)d_ws);
}
```

```cpp
#include <hip/hip_runtime.h>
#include <stdint.h>

#define MK_STAMPS 0
#define HOLD_T 1150

namespace {
typedef unsigned short u16;
typedef unsigned v4u __attribute__((ext_vector_type(4)));
typedef unsigned v2u __attribute__((ext_vector_type(2)));
typedef float f4 __attribute__((ext_vector_type(4)));
typedef float f2 __attribute__((ext_vector_type(2)));
typedef __bf16 bf8 __attribute__((ext_vector_type(8)));
typedef __bf16 bf2 __attribute__((ext_vector_type(2)));
typedef short s4 __attribute__((ext_vector_type(4)));
typedef __amdgpu_buffer_rsrc_t rsrc_t;
#define LAS __attribute__((address_space(3)))

constexpr int NB = 4, D = 640, NH = 32, DH = 128, SK = 4096, WIN = 1024, FF = 1728, HD = 4096;
constexpr unsigned SENT = 0xFFFFFFFFu;
constexpr int OOB = 0x40000000;
constexpr int SZ_Q = 32 * 4 * 256, SZ_O = 32 * 4 * 256, SZ_X1 = 4 * 40 * 128, SZ_H = 108 * 128, SZ_X2 = 40 * 128;
constexpr int OFF_Q = 0, OFF_O = OFF_Q + 2 * SZ_Q, OFF_X1 = OFF_O + 2 * SZ_O, OFF_H = OFF_X1 + 2 * SZ_X1,
              OFF_X2 = OFF_H + 2 * SZ_H, OFF_PROBE = OFF_X2 + SZ_X2, DATA_END = OFF_PROBE + 8 * 128,
              OFF_FLAG = 2 * ((DATA_END + 8191) & ~8191), OFF_EP = OFF_FLAG + 2048, OFF_NEAR = OFF_EP + 128, WS_USED = OFF_NEAR + 128;
__device__ __forceinline__ int dup(int off, int copy) { return off + (off & ~8191) + (copy << 13); }
constexpr int OFF_STAMP = 1 << 20;
constexpr int SPIN_CAP = 1 << 17;
constexpr int L_V = 0;
constexpr int L_P = 131072;
constexpr int L_OW = L_P + 2048;
constexpr int L_ML = L_OW + 4096;
constexpr int L_Q = L_ML + 64;
constexpr int L_QF = L_Q + 256;
constexpr int L_ST = L_QF + 16;
constexpr int L_END = L_ST + (MK_STAMPS ? 1024 : 0);
constexpr int L_RED = 0;
constexpr int RED_W = 5 * 36 * 4;
constexpr int L_NEAR = 6144;

constexpr int AUX_NT = 2, AUX_SC1 = 16;

__device__ __forceinline__ f4 mfma(v4u a, v4u b, f4 c) {
  return __builtin_amdgcn_mfma_f32_16x16x32_bf16(__builtin_bit_cast(bf8, a), __builtin_bit_cast(bf8, b), c, 0, 0, 0);
}
__device__ __forceinline__ unsigned hi16pack(float hi, float lo) {
  return __builtin_amdgcn_perm(__builtin_bit_cast(unsigned, hi), __builtin_bit_cast(unsigned, lo), 0x07060302u);
}
__device__ __forceinline__ unsigned pack_bf16(float lo, float hi) {
  bf2 r = {(__bf16)lo, (__bf16)hi};
  unsigned u = __builtin_bit_cast(unsigned, r);
  return u == SENT ? 0x7FFF7FFFu : u;
}
__device__ __forceinline__ float bflo(unsigned u) { return __builtin_bit_cast(float, u << 16); }
__device__ __forceinline__ float bfhi(unsigned u) { return __builtin_bit_cast(float, u & 0xFFFF0000u); }
__device__ __forceinline__ float dot2(unsigned a, unsigned b, float c) {
  return __builtin_amdgcn_fdot2_f32_bf16(__builtin_bit_cast(bf2, a), __builtin_bit_cast(bf2, b), c, false);
}
template <int CTRL> __device__ __forceinline__ float dpp(float x) {
  return __builtin_bit_cast(float, __builtin_amdgcn_mov_dpp(__builtin_bit_cast(int, x), CTRL, 0xf, 0xf, true));
}
constexpr int XOR1 = 0xB1, XOR2 = 0x4E, ROR4 = 0x124, ROR8 = 0x128, SHL4 = 0x104;
__device__ __forceinline__ float xrow_sum(float x) {
  auto s = __builtin_amdgcn_permlane16_swap(__float_as_uint(x), __float_as_uint(x), false, false);
  x = __uint_as_float(s[0]) + __uint_as_float(s[1]);
  auto t = __builtin_amdgcn_permlane32_swap(__float_as_uint(x), __float_as_uint(x), false, false);
  return __uint_as_float(t[0]) + __uint_as_float(t[1]);
}
__device__ __forceinline__ float xrow_max(float x) {
  auto s = __builtin_amdgcn_permlane16_swap(__float_as_uint(x), __float_as_uint(x), false, false);
  x = fmaxf(__uint_as_float(s[0]), __uint_as_float(s[1]));
  auto t = __builtin_amdgcn_permlane32_swap(__float_as_uint(x), __float_as_uint(x), false, false);
  return fmaxf(__uint_as_float(t[0]), __uint_as_float(t[1]));
}
__device__ __forceinline__ float x32_sum(float x) {
  auto t = __builtin_amdgcn_permlane32_swap(__float_as_uint(x), __float_as_uint(x), false, false);
  return __uint_as_float(t[0]) + __uint_as_float(t[1]);
}
__device__ __forceinline__ rsrc_t mkrsrc(const void* p, unsigned bytes) {
  return __builtin_amdgcn_make_buffer_rsrc((void*)p, 0, bytes, 0x00020000);
}
template <int AUX> __device__ __forceinline__ v4u ld128(rsrc_t rs, int voff, int soff) {
  return __builtin_amdgcn_raw_buffer_load_b128(rs, voff, soff, AUX);
}
__device__ __forceinline__ unsigned umax3(unsigned a, unsigned b, unsigned c) { return max(max(a, b), c); }
__device__ __forceinline__ unsigned vmax4(v4u x, unsigned m) { return umax3(x.x, x.y, umax3(x.z, x.w, m)); }

#define NPROBE 5
#define DLY 11
__device__ __forceinline__ unsigned long long now() { return __builtin_amdgcn_s_memrealtime(); }
__device__ __forceinline__ void hold(unsigned long long t0, int ticks) {
  if (ticks > 0) while ((long long)(now() - t0) < ticks) __builtin_amdgcn_s_sleep(1);
}
#define T_NEAR $((h-130))
#define T_X1_H 70
#define T_WO_X2 265
#define T_GU_X2 140
#define T_Q_O 215
#define T_Q_X3 330
#define T_O_Q 380
#define SETTLE() __builtin_amdgcn_s_sleep(DLY)
__device__ __forceinline__ int xcc_id() { return __builtin_amdgcn_s_getreg(20 | (3 << 11)) & 7; }
__device__ __forceinline__ void near_probe(rsrc_t rws, int slot, unsigned tag, int l) {
  unsigned best0 = ~0u, best1 = ~0u;
#pragma unroll 1
  for (int i = 0; i < NPROBE; ++i) {
    unsigned long long t0, t1, t2;
    const int o0 = l == 0 ? dup(OFF_PROBE + slot * 128, 0) : OOB, o1 = l == 0 ? dup(OFF_PROBE + slot * 128, 1) : OOB;
    asm volatile("s_waitcnt vmcnt(0)\n\ts_memtime %0\n\ts_waitcnt lgkmcnt(0)\n\tbuffer_store_dword %3, %4, %6, 0 offen sc1\n\ts_waitcnt vmcnt(0)\n\t"
                 "s_memtime %1\n\ts_waitcnt lgkmcnt(0)\n\tbuffer_store_dword %3, %5, %6, 0 offen sc1\n\ts_waitcnt vmcnt(0)\n\ts_memtime %2\n\ts_waitcnt lgkmcnt(0)"
                 : "=&s"(t0), "=&s"(t1), "=&s"(t2) : "v"(i), "v"(o0), "v"(o1), "s"(rws) : "memory");
    best0 = min(best0, (unsigned)(t1 - t0)); best1 = min(best1, (unsigned)(t2 - t1));
  }
  if (l == 0) __builtin_amdgcn_raw_buffer_store_b32((tag << 1) | (best1 < best0 ? 1u : 0u), rws, OFF_NEAR + xcc_id() * 4, 0, AUX_SC1);
}
__device__ __forceinline__ int near_fetch(rsrc_t rws, unsigned tag) {
  const unsigned v = __builtin_amdgcn_readfirstlane(__builtin_amdgcn_raw_buffer_load_b32(rws, OFF_NEAR + xcc_id() * 4, 0, AUX_SC1));
  return (v >> 1) == (tag & 0x7FFFFFFFu) ? (int)(v & 1u) : 0;
}
template <int N> __device__ __forceinline__ void gather(v4u (&x)[N], rsrc_t rs, const int (&off_)[N], bool& dead, int nc) {
  int off[N];
#pragma unroll
  for (int i = 0; i < N; ++i) off[i] = dup(off_[i], nc);
  for (int spin = 0;; ++spin) {
    asm volatile("" ::: "memory");
#pragma unroll
    for (int i = 0; i < N; ++i) x[i] = ld128<AUX_SC1>(rs, off[i], 0);
    unsigned m = 0u;
#pragma unroll
    for (int i = 0; i < N; ++i) m = vmax4(x[i], m);
    if (!__any(m == SENT)) break;
    if (dead || spin > SPIN_CAP) { dead = true; break; }
  }
}
__device__ __forceinline__ unsigned long long epoch_load(rsrc_t rws) {
  const v2u e = __builtin_amdgcn_raw_buffer_load_b64(rws, OFF_EP, 0, AUX_SC1);
  return ((unsigned long long)(unsigned)__builtin_amdgcn_readfirstlane(e.y) << 32 | (unsigned)__builtin_amdgcn_readfirstlane(e.x)) + 1ull;
}
__device__ __forceinline__ void clear16(rsrc_t rws, int off) {
  __builtin_amdgcn_raw_buffer_store_b128(v4u{SENT, SENT, SENT, SENT}, rws, dup(off, 0), 0, AUX_SC1);
  __builtin_amdgcn_raw_buffer_store_b128(v4u{SENT, SENT, SENT, SENT}, rws, dup(off, 1), 0, AUX_SC1);
}
__device__ __forceinline__ void flag_post(rsrc_t rws, int cu, unsigned long long tag, int l) {
  asm volatile("s_waitcnt vmcnt(0)" ::: "memory");
  if (l == 0) __builtin_amdgcn_raw_buffer_store_b64(v2u{(unsigned)tag, (unsigned)(tag >> 32)}, rws, OFF_FLAG + cu * 8, 0, AUX_SC1);
}
__device__ __forceinline__ void flags_wait(rsrc_t rws, unsigned long long tag, int l, bool& dead) {
  const unsigned lo = (unsigned)tag, hi = (unsigned)(tag >> 32);
  for (int spin = 0;; ++spin) {
    asm volatile("" ::: "memory");
    const v4u a = ld128<AUX_SC1>(rws, OFF_FLAG + l * 32, 0), b = ld128<AUX_SC1>(rws, OFF_FLAG + l * 32 + 16, 0);
    const bool ok = (a.x == lo) & (a.y == hi) & (a.z == lo) & (a.w == hi) & (b.x == lo) & (b.y == hi) & (b.z == lo) & (b.w == hi);
    if (__all(ok)) break;
    if (dead || spin > SPIN_CAP) { dead = true; break; }
    __builtin_amdgcn_s_sleep(4);
  }
}
__device__ __forceinline__ v4u xpose16(v4u raw, v4u sel1, v4u sel2) {
  const f4 z = {0.f, 0.f, 0.f, 0.f};
  const f4 d1 = mfma(raw, sel1, z), d2 = mfma(raw, sel2, z);
  return v4u{hi16pack(d1[1], d1[0]), hi16pack(d1[3], d1[2]), hi16pack(d2[1], d2[0]), hi16pack(d2[3], d2[2])};
}
__device__ __forceinline__ void xpose32(v4u& a, v4u& b, v4u sel1, v4u sel2) {
  const f4 z = {0.f, 0.f, 0.f, 0.f};
  const f4 a1 = mfma(a, sel1, z), b1 = mfma(b, sel1, z), a2 = mfma(a, sel2, z), b2 = mfma(b, sel2, z);
  a = v4u{hi16pack(a1[1], a1[0]), hi16pack(a1[3], a1[2]), hi16pack(b1[1], b1[0]), hi16pack(b1[3], b1[2])};
  b = v4u{hi16pack(a2[1], a2[0]), hi16pack(a2[3], a2[2]), hi16pack(b2[1], b2[0]), hi16pack(b2[3], b2[2])};
}

#define STAMP(i) do { } while (0)
#define STAMP2(p, a, b) do { } while (0)

#define ASM_LD(dst, voff, rs, soff, MODS) \
  asm volatile("buffer_load_dwordx4 %0, %1, %2, %3 offen " MODS : "=v"(dst) : "v"(voff), "s"(rs), "s"(soff) : "memory")
#define ASM_DMA(voff, rs, soff, ldsa) \
  asm volatile("s_mov_b32 m0, %3\n\ts_nop 0\n\tbuffer_load_dwordx4 %0, %1, %2 offen nt lds" ::"v"(voff), "s"(rs), "s"(soff), "s"(ldsa) : "memory")
#define WAIT_VM(n) asm volatile("s_waitcnt vmcnt(" #n ")" ::: "memory")
#define PIN8(a, o) asm volatile("" : "+v"(a[o]), "+v"(a[o + 1]), "+v"(a[o + 2]), "+v"(a[o + 3]), "+v"(a[o + 4]), "+v"(a[o + 5]), "+v"(a[o + 6]), "+v"(a[o + 7]))

__device__ __forceinline__ v2u tr_read(LAS unsigned char* p) {
  s4 t = __builtin_amdgcn_ds_read_tr16_b64_v4i16((LAS s4*)p);
  return __builtin_bit_cast(v2u, t);
}

__device__ __forceinline__ void attn_math(const int PASS, LAS unsigned char* L, const v4u (&kf)[32], const v4u (&vf)[16], const v4u (&qf)[4],
                                          rsrc_t rws, int b, int h, int w, int l_, LAS unsigned* st) {
  int l = l_; asm volatile("" : "+v"(l));
  const int g = l >> 4;
  const f4 z = {0.f, 0.f, 0.f, 0.f};
  constexpr float CS = 0.08838834764831845f * 1.4426950408889634f;
  float s0 = 0.f, s1 = 0.f;
#pragma unroll
  for (int t = 0; t < 8; t += 2) {
    f4 d0 = z, d1 = z;
#pragma unroll
    for (int s = 0; s < 4; ++s) { d0 = mfma(qf[s], kf[t * 4 + s], d0); d1 = mfma(qf[s], kf[t * 4 + 4 + s], d1); }
    if (t < 4) { s0 = (g == t) ? d0[0] : s0; s0 = (g == t + 1) ? d1[0] : s0; }
    else { s1 = (g == t - 4) ? d0[0] : s1; s1 = (g == t - 3) ? d1[0] : s1; }
  }
  STAMP2(PASS, 16, 8);
  float m = fmaxf(s0, s1);
  m = fmaxf(m, dpp<XOR1>(m)); m = fmaxf(m, dpp<XOR2>(m)); m = fmaxf(m, dpp<ROR4>(m)); m = fmaxf(m, dpp<ROR8>(m));
  m = xrow_max(m);
  const __bf16 p0 = (__bf16)__builtin_amdgcn_exp2f((s0 - m) * CS), p1 = (__bf16)__builtin_amdgcn_exp2f((s1 - m) * CS);
  LAS __bf16* pw = (LAS __bf16*)(L + L_P + w * 256);
  pw[l] = p0; pw[64 + l] = p1;
  v4u pf[4];
#pragma unroll
  for (int s = 0; s < 4; ++s) pf[s] = *(LAS v4u*)(L + L_P + w * 256 + s * 64 + g * 16);
  STAMP2(PASS, 17, 9);
  float ls = (float)p0 + (float)p1;
  ls += dpp<XOR1>(ls); ls += dpp<XOR2>(ls); ls += dpp<ROR4>(ls); ls += dpp<ROR8>(ls);
  ls = xrow_sum(ls);
  float oa0 = 0.f, oa1 = 0.f;
#pragma unroll
  for (int n = 0; n < 8; n += 2) {
    f4 d0 = mfma(pf[0], vf[n], z), d1 = mfma(pf[0], vf[n + 1], z);
    d0 = mfma(pf[1], vf[8 + n], d0); d1 = mfma(pf[1], vf[9 + n], d1);
    if (n < 4) { oa0 = (g == n) ? d0[0] : oa0; oa0 = (g == n + 1) ? d1[0] : oa0; }
    else { oa1 = (g == n - 4) ? d0[0] : oa1; oa1 = (g == n - 3) ? d1[0] : oa1; }
  }
  const int q4 = (l >> 2) & 3;
  const int trb = L_V + w * 16384 + (8 * g + q4) * 256 + ((q4 | ((g & 1) << 2)) << 5) + (l & 3) * 8;
  float ob0 = 0.f, ob1 = 0.f;
#pragma unroll
  for (int hb = 0; hb < 2; ++hb) {
    if (hb == 0) WAIT_VM(8); else WAIT_VM(0);
    if (hb == 1) STAMP2(PASS, 2, 6);
    v2u t0[4], t1[4];
    float pc0 = 0.f, pc1 = 0.f;
#define TR_LOAD(n) do { LAS unsigned char* a_ = L + (trb ^ ((n) << 5)) + hb * 8192; t0[(n) & 3] = tr_read(a_); t1[(n) & 3] = tr_read(a_ + 1024); } while (0)
    TR_LOAD(0); TR_LOAD(1);
#pragma unroll
    for (int n = 0; n < 8; n += 2) {
      if (n < 6) { TR_LOAD(n + 2); TR_LOAD(n + 3); }
      __builtin_amdgcn_sched_barrier(0);
      const f4 d0 = mfma(pf[2 + hb], v4u{t0[n & 3].x, t0[n & 3].y, t1[n & 3].x, t1[n & 3].y}, z);
      const f4 d1 = mfma(pf[2 + hb], v4u{t0[(n + 1) & 3].x, t0[(n + 1) & 3].y, t1[(n + 1) & 3].x, t1[(n + 1) & 3].y}, z);
      if (n < 4) { pc0 = (g == n) ? d0[0] : pc0; pc0 = (g == n + 1) ? d1[0] : pc0; }
      else { pc1 = (g == n - 4) ? d0[0] : pc1; pc1 = (g == n - 3) ? d1[0] : pc1; }
    }
    ob0 += pc0; ob1 += pc1;
  }
  LAS float* ow = (LAS float*)(L + L_OW);
  ow[w * 128 + l] = oa0 + ob0; ow[w * 128 + 64 + l] = oa1 + ob1;
  if (l == 0) *(LAS f2*)(L + L_ML + w * 8) = f2{m, ls};
  STAMP2(PASS, 18, 10);
  __syncthreads();
  STAMP2(PASS, 19, 11);
  if (w == 0) {
    f4 ml[4];
    f2 ov[8];
#pragma unroll
    for (int i = 0; i < 4; ++i) ml[i] = *(LAS f4*)(L + L_ML + i * 16);
#pragma unroll
    for (int i = 0; i < 8; ++i) ov[i] = *(LAS f2*)(L + L_OW + i * 512 + l * 8);
    float M = fmaxf(fmaxf(fmaxf(ml[0][0], ml[0][2]), fmaxf(ml[1][0], ml[1][2])), fmaxf(fmaxf(ml[2][0], ml[2][2]), fmaxf(ml[3][0], ml[3][2])));
    float Ls = 0.f, o0 = 0.f, o1 = 0.f;
#pragma unroll
    for (int i = 0; i < 8; ++i) {
      const float f = __builtin_amdgcn_exp2f((ml[i >> 1][(i & 1) * 2] - M) * CS);
      Ls = fmaf(f, ml[i >> 1][(i & 1) * 2 + 1], Ls); o0 = fmaf(f, ov[i].x, o0); o1 = fmaf(f, ov[i].y, o1);
    }
    const float inv = __builtin_amdgcn_rcpf(Ls);
    const unsigned ob = pack_bf16(o0 * inv, o1 * inv);
    const int oo = OFF_O + PASS * SZ_O + (h * 4 + b) * 256 + l * 4;
    __builtin_amdgcn_raw_buffer_store_b32(ob, rws, dup(oo, 0), 0, AUX_SC1);
    __builtin_amdgcn_raw_buffer_store_b32(ob, rws, dup(oo, 1), 0, AUX_SC1);
  }
  STAMP2(PASS, 3, 7);
}

__device__ __forceinline__ void attn_role(LAS unsigned char* L, const u16* K, const u16* V, rsrc_t rws, int cu, int xc, int y, int w, int l,
                                          LAS unsigned* st) {
  const int h = 4 * xc + (y >> 2), b = y & 3, g = l >> 4, r = l & 15;
  const size_t kv = ((size_t)(b * NH + h) * SK + (SK - WIN) + 128 * w) * DH;
  const rsrc_t rK = mkrsrc(K + kv, 32768), rV = mkrsrc(V + kv, 32768);
  bool dead = false;
  v4u kf[32], vf[16], qf[4];
  const int ldsV = L_V + w * 16384;
  const int ks0 = g * 256 + ((r ^ g) << 4);
#define KX(i) (((i) & 3) << 6)
  const int vs0 = g * 256 + (((r >> 1) ^ g) << 5) + (l & 1) * 16, vs1 = g * 256 + (((r >> 1) ^ (g | 4)) << 5) + (l & 1) * 16;
#define K_PIECE(n) _Pragma("unroll") for (int i = 0; i < 8; ++i) \
    ASM_DMA(ks0 ^ KX(i), rK, (n) * 8192 + i * 1024, ldsV + ((n) & 1) * 8192 + i * 1024)
#define V_PIECE(n, buf) _Pragma("unroll") for (int i = 0; i < 8; ++i) \
    ASM_DMA((i & 2) ? vs1 : vs0, rV, (n) * 8192 + i * 1024, ldsV + (buf) * 8192 + i * 1024)
#define K_READ(n) do { _Pragma("unroll") for (int t = 0; t < 2; ++t) _Pragma("unroll") for (int s = 0; s < 4; ++s) \
      kf[((n) * 2 + t) * 4 + s] = *(LAS v4u*)(L + ldsV + ((n) & 1) * 8192 + (16 * t + r) * 256 + (((4 * s + g) ^ r) << 4)); \
    asm volatile("s_waitcnt lgkmcnt(0)" ::: "memory"); PIN8(kf, (n) * 8); } while (0)
  const int trb = ldsV + (8 * g + ((l >> 2) & 3)) * 256 + ((((l >> 2) & 3) | ((g & 1) << 2)) << 5) + (l & 3) * 8;
#define V_READ(n) do { _Pragma("unroll") for (int c8 = 0; c8 < 8; ++c8) { \
      LAS unsigned char* a = L + (trb ^ (c8 << 5)) + (n) * 8192; \
      const v2u t0 = tr_read(a), t1 = tr_read(a + 1024); \
      vf[(n) * 8 + c8] = v4u{t0.x, t0.y, t1.x, t1.y}; } \
    asm volatile("s_waitcnt lgkmcnt(0)" ::: "memory"); PIN8(vf, (n) * 8); } while (0)
  K_PIECE(0);
  if (threadIdx.x == 0) *(volatile LAS unsigned*)(L + L_QF) = 0u;
  asm volatile("s_waitcnt lgkmcnt(0)\n\ts_barrier" ::: "memory");
  v2u ep = {0u, 0u};
  unsigned long long tag = 0;
  {
    const v4u sent = {SENT, SENT, SENT, SENT};
    const int co = dup(OFF_O + ((l >> 4) & 1) * SZ_O + (h * 4 + b) * 256 + r * 16, l >> 5);
    asm volatile("s_cmp_eq_u32 %5, 0\n\ts_cbranch_scc0 1f\n\tbuffer_load_dwordx2 %0, %1, %4, 0 offen sc1\n\t"
                 "buffer_store_dwordx4 %2, %3, %4, 0 offen sc1\n\ts_nop 1\n1:"
                 : "+v"(ep) : "v"(OFF_EP), "v"(sent), "v"(co), "s"(rws), "s"(w) : "memory", "scc");
  }
  K_PIECE(1);
  WAIT_VM(8);
  asm volatile("" : "+v"(ep));
  if (w == 0) {
    tag = ((unsigned long long)(unsigned)__builtin_amdgcn_readfirstlane(ep.y) << 32 | (unsigned)__builtin_amdgcn_readfirstlane(ep.x)) + 1ull;
    const v2u tg = {(unsigned)tag, (unsigned)(tag >> 32)};
    asm volatile("buffer_store_dwordx2 %0, %1, %2, 0 offen sc1\n\ts_nop 0" ::"v"(tg), "v"(l == 0 ? OFF_FLAG + cu * 8 : OOB), "s"(rws) : "memory");
  }
  K_READ(0); K_PIECE(2);
  WAIT_VM(8); K_READ(1); K_PIECE(3);
  WAIT_VM(8); K_READ(2); V_PIECE(0, 0);
  WAIT_VM(8); K_READ(3); V_PIECE(1, 1);
  if (w == 0) {
    flags_wait(rws, tag, l, dead);
    v4u q1[1];
    const int off[1] = {l < 16 ? OFF_Q + (h * 16 + b) * 64 + (l >> 2) * 256 + (l & 3) * 16 : OOB};
    gather<1>(q1, rws, off, dead, 0);
    if (l < 16) *(LAS v4u*)(L + L_Q + l * 16) = q1[0];
    asm volatile("s_waitcnt lgkmcnt(0)" ::: "memory");
    if (l == 0) *(volatile LAS unsigned*)(L + L_QF) = 1u;
  }
  WAIT_VM(8); V_READ(0); V_PIECE(2, 0);
  WAIT_VM(8); V_READ(1); V_PIECE(3, 1);
  STAMP(1);
#pragma unroll 1
  for (int pass = 0; pass < 2; ++pass) {
    if (pass == 0) {
      for (int spin = 0; *(volatile LAS unsigned*)(L + L_QF) == 0u; ++spin) {
        if (spin > (SPIN_CAP << 4)) { dead = true; break; }
        __builtin_amdgcn_s_sleep(1);
      }
#pragma unroll
      for (int s = 0; s < 4; ++s) qf[s] = *(LAS v4u*)(L + L_Q + s * 64 + g * 16);
      asm volatile("" ::: "memory");
    } else {
      const unsigned long long tr = now();
      if (w == 0) { const int n_ = near_fetch(rws, (unsigned)tag); if (l == 0) *(volatile LAS unsigned*)(L + L_QF + 4) = n_; }
      __syncthreads();
      const int nc = __builtin_amdgcn_readfirstlane(*(volatile LAS unsigned*)(L + L_QF + 4));
      hold(tr, T_O_Q);
      int lo = l; asm volatile("" : "+v"(lo));
      const int qo = OFF_Q + SZ_Q + (h * 16 + b) * 64 + (lo >> 4) * 16;
      const int off[4] = {qo, qo + 256, qo + 512, qo + 768};
      gather<4>(qf, rws, off, dead, nc);
    }
    STAMP2(pass, 4, 5);
    attn_math(pass, L, kf, vf, qf, rws, b, h, w, l, st);
  }
}

__device__ __forceinline__ f4 red_sum(LAS unsigned char* L, int row, int l) {
  const int hf = l >> 5, cg = l & 7;
  f4 s = *(LAS f4*)(L + L_RED + (hf * 4) * RED_W + row * 144 + cg * 16);
#pragma unroll
  for (int i = 1; i < 4; ++i) s += *(LAS f4*)(L + L_RED + (hf * 4 + i) * RED_W + row * 144 + cg * 16);
  return f4{x32_sum(s[0]), x32_sum(s[1]), x32_sum(s[2]), x32_sum(s[3])};
}
__device__ __forceinline__ f2 red_stat(LAS unsigned char* L, int row, int l) {
  const int hf = l >> 5;
  f2 s = *(LAS f2*)(L + L_RED + (hf * 4) * RED_W + row * 144 + 128);
#pragma unroll
  for (int i = 1; i < 4; ++i) s += *(LAS f2*)(L + L_RED + (hf * 4 + i) * RED_W + row * 144 + 128);
  return f2{x32_sum(s[0]), x32_sum(s[1])};
}

__device__ __forceinline__ f4 red_sum_gu(LAS unsigned char* L, int row, int l) {
  const int m = l >> 5, cg = l & 7;
  f4 s = *(LAS f4*)(L + L_RED + m * RED_W + row * 144 + cg * 16);
#pragma unroll
  for (int i = 1; i < 4; ++i) s += *(LAS f4*)(L + L_RED + (2 * i + m) * RED_W + row * 144 + cg * 16);
  return s;
}
struct WArgs { const u16 *x, *wq, *wo, *wg, *wu, *wd; u16* out; };

template <int PASS>
__device__ __forceinline__ void q_phase(LAS unsigned char* L, const v4u (&xf)[3], const v4u (&wqf)[6], f4 csq, rsrc_t rws, int tq, int w, int l, LAS unsigned* st) {
  const int g = l >> 4, c = l & 15;
  const f4 z = {0.f, 0.f, 0.f, 0.f};
  const v4u ones = {0x3F803F80u, 0x3F803F80u, 0x3F803F80u, 0x3F803F80u};
  f4 d0 = z, d1 = z, ds = z;
  float ss = 0.f;
#pragma unroll
  for (int i = 0; i < 3; ++i) {
    d0 = mfma(wqf[2 * i], xf[i], d0); d1 = mfma(wqf[2 * i + 1], xf[i], d1); ds = mfma(ones, xf[i], ds);
    ss = dot2(xf[i].x, xf[i].x, ss); ss = dot2(xf[i].y, xf[i].y, ss); ss = dot2(xf[i].z, xf[i].z, ss); ss = dot2(xf[i].w, xf[i].w, ss);
  }
  ss = xrow_sum(ss);
  LAS unsigned char* R = L + L_RED + w * RED_W + c * 144;
  if (c < 4) {
    *(LAS f4*)(R + g * 16) = d0; *(LAS f4*)(R + 64 + g * 16) = d1;
    if (g == 0) *(LAS f2*)(R + 128) = f2{ds[0], ss};
  }
  __syncthreads();
  if (PASS == 1) STAMP(22);
  if (w == 0) {
    const int b = (l >> 3) & 3, cg = l & 7;
    const f4 p = red_sum(L, b, l);
    const f2 s = red_stat(L, b, l);
    const float mu = s[0] * (1.0f / D), var = fmaxf(s[1] * (1.0f / D) - mu * mu, 0.f), rs = __builtin_amdgcn_rsqf(var + 1e-5f);
    {
      const v2u o = {pack_bf16((p[0] - mu * csq[0]) * rs, (p[1] - mu * csq[1]) * rs), pack_bf16((p[2] - mu * csq[2]) * rs, (p[3] - mu * csq[3]) * rs)};
      __builtin_amdgcn_raw_buffer_store_b64(o, rws, dup(OFF_Q + PASS * SZ_Q + tq * 256 + b * 64 + cg * 8, l >> 5), 0, AUX_SC1);
    }
  }
  __syncthreads();
}

__device__ __forceinline__ void q_second(LAS unsigned char* L, const v4u (&wqf)[6], f4 csq, rsrc_t rws, int tq, int w, int l, bool& dead, int nc,
                                         LAS unsigned* st) {
  const int g = l >> 4, c = l & 15;
  v4u xf[3];
  int off[3];
#pragma unroll
  for (int i = 0; i < 3; ++i) {
    const int s = w + 8 * i;
    off[i] = (c < 4 && s < 20) ? OFF_X2 + ((2 * s + (g >> 1)) * 4 + c) * 32 + (g & 1) * 16 : OOB;
  }
  gather<3>(xf, rws, off, dead, nc);
  STAMP(6);
  q_phase<1>(L, xf, wqf, csq, rws, tq, w, l, st);
  STAMP(7);
}

__device__ __forceinline__ void weight_role(LAS unsigned char* L, const WArgs& a, rsrc_t rws, int cu, int xc, int u, int w, int l,
                                            LAS unsigned* st) {
  const int g = l >> 4, r = l & 15, c = r;
  const f4 z = {0.f, 0.f, 0.f, 0.f};
  const v4u ones = {0x3F803F80u, 0x3F803F80u, 0x3F803F80u, 0x3F803F80u};
  v4u sel1, sel2;
  {
    const int j = r - 8 * (g & 1);
    const unsigned one = (j & 1) ? 0x3F800000u : 0x00003F80u;
    const bool in = (unsigned)j < 8u;
    v4u s = {(in && (j >> 1) == 0) ? one : 0u, (in && (j >> 1) == 1) ? one : 0u, (in && (j >> 1) == 2) ? one : 0u, (in && (j >> 1) == 3) ? one : 0u};
    const v4u zz = {0u, 0u, 0u, 0u};
    sel1 = g < 2 ? s : zz; sel2 = g < 2 ? zz : s;
  }
  const int tq = xc * 16 + u;
  const bool has_wo = u < 10, has_gu = u < (xc < 3 ? 8 : 6), has_dn = u >= 10 && u < 15;
  const int wo_id = xc * 10 + u, kq = wo_id / 20, nt32 = wo_id % 20;
  const int t32 = xc < 3 ? xc * 8 + u : 24 + (xc - 3) * 6 + u;
  const int td = xc * 5 + (u - 10);
  const int rho16 = 8 * (r >> 2) + (r & 3);
  const int rho32 = rho16 + 4 * (g >> 1);

  bool dead = false;
  const unsigned long long tstart = __builtin_amdgcn_s_memrealtime();
  v2u ep = {0u, 0u};
  if (w == 0) {
    ep = __builtin_amdgcn_raw_buffer_load_b64(rws, OFF_EP, 0, AUX_SC1);
    const int hf = l >> 5, k = l & 31;
    clear16(rws, k < 16 ? OFF_Q + hf * SZ_Q + tq * 256 + k * 16 : has_wo ? OFF_X1 + hf * SZ_X1 + (kq * 40 + 2 * nt32) * 128 + (k - 16) * 16 : OOB);
    clear16(rws, (k < 16 && has_gu) ? OFF_H + hf * SZ_H + t32 * 256 + k * 16 : (k < 8 && hf == 0 && has_dn) ? OFF_X2 + td * 128 + k * 16 : OOB);
  }
  v4u wqf[6];
  unsigned long long tag = 0;
  f4 csq;
  {
    v4u xf[3];
    const rsrc_t rx = mkrsrc(a.x, NB * D * 2), rq = mkrsrc(a.wq, D * HD * 2);
#pragma unroll
    for (int i = 0; i < 3; ++i) {
      const int s = w + 8 * i;
      xf[i] = ld128<0>(rx, (c < 4 && s < 20) ? c * 1280 + s * 64 + g * 16 : OOB, 0);
    }
#pragma unroll
    for (int i = 0; i < 3; ++i) {
      const int s = w + 8 * i;
      const int vo = s < 20 ? (32 * s + rho16) * 8192 + tq * 64 + g * 16 : OOB;
      wqf[2 * i] = ld128<AUX_NT>(rq, vo, 0); wqf[2 * i + 1] = ld128<AUX_NT>(rq, vo, 4 * 8192);
    }
    __builtin_amdgcn_sched_barrier(0);
    if (w == 0) {
      asm volatile("s_waitcnt vmcnt(9)" : "+v"(ep)::"memory");
      tag = ((unsigned long long)(unsigned)__builtin_amdgcn_readfirstlane(ep.y) << 32 | (unsigned)__builtin_amdgcn_readfirstlane(ep.x)) + 1ull;
      if (l == 0) __builtin_amdgcn_raw_buffer_store_b64(v2u{(unsigned)tag, (unsigned)(tag >> 32)}, rws, OFF_FLAG + cu * 8, 0, AUX_SC1);
    }
#pragma unroll
    for (int i = 0; i < 3; ++i) xpose32(wqf[2 * i], wqf[2 * i + 1], sel1, sel2);
    f4 c0 = z, c1 = z;
#pragma unroll
    for (int i = 0; i < 3; ++i) { c0 = mfma(wqf[2 * i], ones, c0); c1 = mfma(wqf[2 * i + 1], ones, c1); }
    LAS unsigned char* R4 = L + L_RED + w * RED_W + 4 * 144;
    if (c == 0) { *(LAS f4*)(R4 + g * 16) = c0; *(LAS f4*)(R4 + 64 + g * 16) = c1; }
    __syncthreads();
    csq = red_sum(L, 4, l);
    q_phase<0>(L, xf, wqf, csq, rws, tq, w, l, st);
  }
  STAMP(1);
  const int fb = (l >> 3) & 3, fcg = l & 7;
  LAS unsigned char* R = L + L_RED + w * RED_W + c * 144;
#define HOLD() do { while ((long long)(__builtin_amdgcn_s_memrealtime() - tstart) < HOLD_T) __builtin_amdgcn_s_sleep(16); } while (0)

  if (has_wo) {
    v4u wof[8], guf[10];
    const int gm = w & 1, gq = w >> 1;
    {
      const rsrc_t ro = mkrsrc(a.wo, D * HD * 2), rgu = mkrsrc(gm ? a.wu : a.wg, D * FF * 2);
      const int vo = ((8 * kq + w) * 128 + rho16) * 1280 + nt32 * 64 + g * 16;
#pragma unroll
      for (int s = 0; s < 4; ++s) { wof[2 * s] = ld128<AUX_NT>(ro, vo, s * 32 * 1280); wof[2 * s + 1] = ld128<AUX_NT>(ro, vo, (s * 32 + 4) * 1280); }
      const int vg = has_gu ? (160 * gq + rho16) * 3456 + t32 * 64 + g * 16 : OOB;
#pragma unroll
      for (int i = 0; i < 5; ++i) { guf[2 * i] = ld128<AUX_NT>(rgu, vg, i * 32 * 3456); guf[2 * i + 1] = ld128<AUX_NT>(rgu, vg, (i * 32 + 4) * 3456); }
      __builtin_amdgcn_sched_barrier(0);
    }
#pragma unroll
    for (int s = 0; s < 4; ++s) xpose32(wof[2 * s], wof[2 * s + 1], sel1, sel2);
#pragma unroll
    for (int i = 0; i < 5; ++i) xpose32(guf[2 * i], guf[2 * i + 1], sel1, sel2);
    f4 csg;
    {
      f4 c0 = z, c1 = z;
#pragma unroll
      for (int i = 0; i < 5; ++i) { c0 = mfma(guf[2 * i], ones, c0); c1 = mfma(guf[2 * i + 1], ones, c1); }
      LAS unsigned char* R4 = L + L_RED + w * RED_W + 4 * 144;
      if (c == 0) { *(LAS f4*)(R4 + g * 16) = c0; *(LAS f4*)(R4 + 64 + g * 16) = c1; }
      if (w == 0) flags_wait(rws, tag, l, dead);
      __syncthreads();
      csg = red_sum_gu(L, 4, l);
    }
    STAMP(2);
    hold(tstart, T_NEAR);
    if (w == 0) { const int n_ = near_fetch(rws, (unsigned)tag); if (l == 0) *(volatile LAS unsigned*)(L + L_NEAR) = n_; }
    __syncthreads();
    const int nc = __builtin_amdgcn_readfirstlane(*(volatile LAS unsigned*)(L + L_NEAR));
    HOLD();
#pragma unroll 1
    for (int pass = 0; pass < 2; ++pass) {
      {
        v4u of[4];
        int lo = l; asm volatile("" : "+v"(lo));
        const int o0 = (lo & 15) < 4 ? OFF_O + pass * SZ_O + ((8 * kq + w) * 4 + (lo & 15)) * 256 + (lo >> 4) * 16 : OOB;
        const int off[4] = {o0, o0 + 64, o0 + 128, o0 + 192};
        v2u res = {0u, 0u};
        if (w == 0 && kq == 0) {
          if (pass == 0) res = *(const v2u*)(a.x + fb * D + nt32 * 32 + fcg * 4);
          else res = __builtin_amdgcn_raw_buffer_load_b64(rws, dup(OFF_X2 + ((2 * nt32 + (fcg >> 2)) * 4 + fb) * 32 + (fcg & 3) * 8, nc), 0, AUX_SC1);
        }
        gather<4>(of, rws, off, dead, nc);
        STAMP2(pass, 3, 9);
        f4 d0 = z, d1 = z;
#pragma unroll
        for (int s = 0; s < 4; ++s) { d0 = mfma(wof[2 * s], of[s], d0); d1 = mfma(wof[2 * s + 1], of[s], d1); }
        if (c < 4) { *(LAS f4*)(R + g * 16) = d0; *(LAS f4*)(R + 64 + g * 16) = d1; }
        __syncthreads();
        STAMP2(pass, 16, 24);
        if (w == 0) {
          f4 p = red_sum(L, fb, l);
          p[0] += bflo(res.x); p[1] += bfhi(res.x); p[2] += bflo(res.y); p[3] += bfhi(res.y);
          {
            const v2u o = {pack_bf16(p[0], p[1]), pack_bf16(p[2], p[3])};
            __builtin_amdgcn_raw_buffer_store_b64(o, rws, dup(OFF_X1 + pass * SZ_X1 + ((kq * 40 + 2 * nt32 + (fcg >> 2)) * 4 + fb) * 32 + (fcg & 3) * 8, l >> 5), 0, AUX_SC1);
          }
          STAMP2(pass, 17, 25);
        }
        __syncthreads();
      }
      if (has_gu) {
        v4u x1[5];
        int xo[5];
        int lo = l; asm volatile("" : "+v"(lo));
#pragma unroll
        for (int i = 0; i < 5; ++i)
          xo[i] = OFF_X1 + pass * SZ_X1 + ((((lo & 15) >> 2) * 40 + 2 * (5 * gq + i) + (lo >> 5)) * 4 + (lo & 3)) * 32 + ((lo >> 4) & 1) * 16;
        SETTLE();
        gather<5>(x1, rws, xo, dead, nc);
        STAMP2(pass, 4, 10);
        f4 d0 = z, d1 = z, ds = z;
#pragma unroll
        for (int i = 0; i < 5; ++i) { d0 = mfma(guf[2 * i], x1[i], d0); d1 = mfma(guf[2 * i + 1], x1[i], d1); }
        if (gm == 0) {
#pragma unroll
          for (int i = 0; i < 5; ++i) ds = mfma(ones, x1[i], ds);
        }
#pragma unroll
        for (int i = 0; i < 4; ++i) {
          d0[i] += dpp<ROR4>(d0[i]); d0[i] += dpp<ROR8>(d0[i]);
          d1[i] += dpp<ROR4>(d1[i]); d1[i] += dpp<ROR8>(d1[i]);
        }
        float sx = ds[0]; sx += dpp<ROR4>(sx); sx += dpp<ROR8>(sx);
        if (c < 4) {
          *(LAS f4*)(R + g * 16) = d0; *(LAS f4*)(R + 64 + g * 16) = d1;
          if (g == 0) *(LAS float*)(R + 128) = sx;
        }
        __syncthreads();
        STAMP2(pass, 18, 26);
        if (w == 0) {
          const f4 p = red_sum_gu(L, fb, l);
          float sm = 0.f;
#pragma unroll
          for (int i = 0; i < 4; ++i) sm += *(LAS float*)(L + L_RED + 2 * i * RED_W + fb * 144 + 128);
          const float mu = sm * (1.0f / D);
          float hv[4];
#pragma unroll
          for (int i = 0; i < 4; ++i) {
            const float v = p[i] - mu * csg[i];
            auto sw = __builtin_amdgcn_permlane32_swap(__float_as_uint(v), __float_as_uint(v), false, false);
            hv[i] = fmaxf(__uint_as_float(sw[0]), 0.f) * __uint_as_float(sw[1]);
          }
          {
            const v2u o = {pack_bf16(hv[0], hv[1]), pack_bf16(hv[2], hv[3])};
            __builtin_amdgcn_raw_buffer_store_b64(o, rws, dup(OFF_H + pass * SZ_H + ((2 * t32 + (fcg >> 2)) * 4 + fb) * 32 + (fcg & 3) * 8, l >> 5), 0, AUX_SC1);
          }
          STAMP2(pass, 19, 27);
        }
        __syncthreads();
      }
      if (pass == 0) { hold(now(), has_gu ? T_GU_X2 : T_WO_X2); q_second(L, wqf, csq, rws, tq, w, l, dead, nc, st); hold(now(), T_Q_O); }
    }
  } else {
    v4u wdf[7];
    if (!has_dn && w == 0) near_probe(rws, xc, (unsigned)tag, l);
#define T_DN_LOAD 600
    hold(tstart, T_DN_LOAD);
    {
      const rsrc_t rd = mkrsrc(a.wd, D * FF * 2);
#pragma unroll
      for (int i = 0; i < 7; ++i) {
        const int s = w + 8 * i;
        wdf[i] = ld128<AUX_NT>(rd, (has_dn && s < 54) ? (32 * s + rho32) * 1280 + td * 32 + (g & 1) * 16 : OOB, 0);
      }
      __builtin_amdgcn_sched_barrier(0);
    }
#pragma unroll
    for (int i = 0; i < 7; ++i) wdf[i] = xpose16(wdf[i], sel1, sel2);
    if (w == 0) {
      flags_wait(rws, tag, l, dead);
      if (has_dn && td == 0 && l == 0) __builtin_amdgcn_raw_buffer_store_b64(v2u{(unsigned)tag, (unsigned)(tag >> 32)}, rws, OFF_EP, 0, AUX_SC1);
    }
    __syncthreads();
    STAMP(2);
    hold(tstart, T_NEAR);
    if (w == 0) { const int n_ = near_fetch(rws, (unsigned)tag); if (l == 0) *(volatile LAS unsigned*)(L + L_NEAR) = n_; }
    __syncthreads();
    const int nc = __builtin_amdgcn_readfirstlane(*(volatile LAS unsigned*)(L + L_NEAR));
    HOLD();
#pragma unroll 1
    for (int pass = 0; pass < 2; ++pass) {
      if (has_dn) {
        v4u x1[3];
        int xoff[3];
        int lo = l; asm volatile("" : "+v"(lo));
#pragma unroll
        for (int i = 0; i < 3; ++i) {
          const int s = w + 8 * i;
          xoff[i] = s < 20 ? OFF_X1 + pass * SZ_X1 + ((((lo & 15) >> 2) * 40 + 2 * s + (lo >> 5)) * 4 + (lo & 3)) * 32 + ((lo >> 4) & 1) * 16 : OOB;
        }
        gather<3>(x1, rws, xoff, dead, nc);
        const unsigned long long tx1 = now();
        STAMP2(pass, 4, 10);
        f4 gram = z, ds = z;
#pragma unroll
        for (int i = 0; i < 3; ++i) { gram = mfma(x1[i], x1[i], gram); ds = mfma(ones, x1[i], ds); }
        float sq = (c & 3) == 0 ? gram[0] : (c & 3) == 1 ? gram[1] : (c & 3) == 2 ? gram[2] : gram[3];
        sq += dpp<ROR4>(sq); sq += dpp<ROR8>(sq); sq = xrow_sum(sq);
        float sx = ds[0]; sx += dpp<ROR4>(sx); sx += dpp<ROR8>(sx);
        if (c < 4 && g == 0) *(LAS f2*)(R + 128) = f2{sx, sq};
        __syncthreads();
        float rs2 = 0.f; f4 res = z;
        if (w == 0) {
          v2u rp[4];
#pragma unroll
          for (int k = 0; k < 4; ++k)
            rp[k] = __builtin_amdgcn_raw_buffer_load_b64(rws, dup(OFF_X1 + pass * SZ_X1 + ((k * 40 + td) * 4 + fb) * 32 + (fcg & 3) * 8, nc), 0, AUX_SC1);
          const f2 s = red_stat(L, fb, l);
          const float mu = s[0] * (1.0f / D), var = fmaxf(s[1] * (1.0f / D) - mu * mu, 0.f);
          rs2 = __builtin_amdgcn_rcpf(var + 1e-5f);
#pragma unroll
          for (int k = 0; k < 4; ++k) { res[0] += bflo(rp[k].x); res[1] += bfhi(rp[k].x); res[2] += bflo(rp[k].y); res[3] += bfhi(rp[k].y); }
        }
        v4u hf[7];
        int hoff[7];
#pragma unroll
        for (int i = 0; i < 7; ++i) {
          const int s = w + 8 * i;
          hoff[i] = ((lo & 15) < 4 && s < 54) ? OFF_H + pass * SZ_H + ((2 * s + (lo >> 5)) * 4 + (lo & 15)) * 32 + ((lo >> 4) & 1) * 16 : OOB;
        }
        hold(tx1, T_X1_H);
        gather<7>(hf, rws, hoff, dead, nc);
        STAMP2(pass, 5, 11);
        f4 d = z;
#pragma unroll
        for (int i = 0; i < 7; ++i) d = mfma(wdf[i], hf[i], d);
        if (c < 4) { *(LAS f4*)(R + g * 16) = d; *(LAS f4*)(R + 64 + g * 16) = z; }
        __syncthreads();
        STAMP2(pass, 20, 28);
        if (w == 0) {
          const f4 p = red_sum(L, fb, l);
          if (fcg < 4) {
            f4 o;
#pragma unroll
            for (int i = 0; i < 4; ++i) o[i] = res[i] + rs2 * p[i];
            if (dead) o = f4{__builtin_nanf(""), __builtin_nanf(""), __builtin_nanf(""), __builtin_nanf("")};
            const v2u ob = {pack_bf16(o[0], o[1]), pack_bf16(o[2], o[3])};
            if (pass == 0) __builtin_amdgcn_raw_buffer_store_b64(ob, rws, dup(OFF_X2 + (td * 4 + fb) * 32 + fcg * 8, l >> 5), 0, AUX_SC1);
            else if (l < 32) *(v2u*)(a.out + fb * D + td * 16 + fcg * 4) = ob;
          }
          STAMP2(pass, 21, 29);
        }
        __syncthreads();
      }
      if (pass == 0) { if (has_dn) SETTLE(); q_second(L, wqf, csq, rws, tq, w, l, dead, nc, st); hold(now(), T_Q_X3); }
    }
  }
}

__global__ __launch_bounds__(512, 2) void mega_kernel(const u16* __restrict__ x, const u16* __restrict__ K, const u16* __restrict__ V,
                                                      const u16* __restrict__ wq, const u16* __restrict__ wo, const u16* __restrict__ wg,
                                                      const u16* __restrict__ wu, const u16* __restrict__ wd, u16* __restrict__ out,
                                                      unsigned char* __restrict__ ws) {
  asm volatile("" ::"s"(x), "s"(K), "s"(V), "s"(wq), "s"(wo), "s"(wg), "s"(wu), "s"(wd), "s"(out), "s"(ws));
  __shared__ __attribute__((aligned(16))) unsigned char lds[L_END];
  LAS unsigned char* L = (LAS unsigned char*)lds;
  const int cu = blockIdx.x, xc = cu & 7, y = cu >> 3;
  const int l = threadIdx.x & 63, w = __builtin_amdgcn_readfirstlane(threadIdx.x >> 6);
  const rsrc_t rws = mkrsrc(ws, WS_USED);
  LAS unsigned* st = (LAS unsigned*)(L + L_ST) + w * 32;
  if (y < 16) attn_role(L, K, V, rws, cu, xc, y, w, l, st);
  else {
    WArgs a{x, wq, wo, wg, wu, wd, out};
    weight_role(L, a, rws, cu, xc, y - 16, w, l, st);
  }
}
}

extern "C" void kernel_launch(void* const* d_in, const int* in_sizes, int n_in, void* d_out, int out_size, void* d_ws, size_t ws_size,
                              hipStream_t stream) {
  (void)in_sizes; (void)n_in; (void)out_size; (void)ws_size;
  mega_kernel<<<256, 512, 0, stream>>>((const u16*)d_in[0], (const u16*)d_in[1], (const u16*)d_in[2], (const u16*)d_in[3],
                                       (const u16*)d_in[4], (const u16*)d_in[6], (const u16*)d_in[7], (const u16*)d_in[8],
                                       (u16*)d_out, (unsigned char*)d_ws);
}
```

```cpp
#include <hip/hip_runtime.h>
#include <stdint.h>

#define OUT_SC1 16
#define HOLD_T 1150

namespace {
typedef unsigned short u16;
typedef unsigned v4u __attribute__((ext_vector_type(4)));
typedef unsigned v2u __attribute__((ext_vector_type(2)));
typedef float f4 __attribute__((ext_vector_type(4)));
typedef float f2 __attribute__((ext_vector_type(2)));
typedef __bf16 bf8 __attribute__((ext_vector_type(8)));
typedef __bf16 bf2 __attribute__((ext_vector_type(2)));
typedef short s4 __attribute__((ext_vector_type(4)));
typedef __amdgpu_buffer_rsrc_t rsrc_t;
#define LAS __attribute__((address_space(3)))

constexpr int NB = 4, D = 640, NH = 32, DH = 128, SK = 4096, WIN = 1024, FF = 1728, HD = 4096;
constexpr unsigned SENT = 0xFFFFFFFFu;
constexpr int OOB = 0x40000000;
constexpr int SZ_Q = 32 * 4 * 256, SZ_O = 32 * 4 * 256, SZ_X1 = 4 * 40 * 128, SZ_H = 108 * 128, SZ_X2 = 40 * 128;
constexpr int OFF_Q = 0, OFF_O = OFF_Q + 2 * SZ_Q, OFF_X1 = OFF_O + 2 * SZ_O, OFF_H = OFF_X1 + 2 * SZ_X1,
              OFF_X2 = OFF_H + 2 * SZ_H, OFF_PROBE = OFF_X2 + SZ_X2, DATA_END = OFF_PROBE + 8 * 128,
              OFF_FLAG = 2 * ((DATA_END + 8191) & ~8191), OFF_EP = OFF_FLAG + 2048, OFF_NEAR = OFF_EP + 128, WS_USED = OFF_NEAR + 128;
__device__ __forceinline__ int dup(int off, int copy) { return off + (off & ~8191) + (copy << 13); }
constexpr int SPIN_CAP = 1 << 17;
constexpr int L_V = 0;
constexpr int L_P = 131072;
constexpr int L_OW = L_P + 2048;
constexpr int L_ML = L_OW + 4096;
constexpr int L_Q = L_ML + 64;
constexpr int L_QF = L_Q + 256;
constexpr int L_END = L_QF + 16;
constexpr int L_RED = 0;
constexpr int RED_W = 5 * 36 * 4;
constexpr int L_NEAR = 6144;

constexpr int AUX_NT = 2, AUX_SC1 = 16;

__device__ __forceinline__ f4 mfma(v4u a, v4u b, f4 c) {
  return __builtin_amdgcn_mfma_f32_16x16x32_bf16(__builtin_bit_cast(bf8, a), __builtin_bit_cast(bf8, b), c, 0, 0, 0);
}
__device__ __forceinline__ unsigned hi16pack(float hi, float lo) {
  return __builtin_amdgcn_perm(__builtin_bit_cast(unsigned, hi), __builtin_bit_cast(unsigned, lo), 0x07060302u);
}
__device__ __forceinline__ unsigned pack_bf16(float lo, float hi) {
  bf2 r = {(__bf16)lo, (__bf16)hi};
  unsigned u = __builtin_bit_cast(unsigned, r);
  return u == SENT ? 0x7FFF7FFFu : u;
}
__device__ __forceinline__ float bflo(unsigned u) { return __builtin_bit_cast(float, u << 16); }
__device__ __forceinline__ float bfhi(unsigned u) { return __builtin_bit_cast(float, u & 0xFFFF0000u); }
__device__ __forceinline__ float dot2(unsigned a, unsigned b, float c) {
  return __builtin_amdgcn_fdot2_f32_bf16(__builtin_bit_cast(bf2, a), __builtin_bit_cast(bf2, b), c, false);
}
template <int CTRL> __device__ __forceinline__ float dpp(float x) {
  return __builtin_bit_cast(float, __builtin_amdgcn_mov_dpp(__builtin_bit_cast(int, x), CTRL, 0xf, 0xf, true));
}
constexpr int XOR1 = 0xB1, XOR2 = 0x4E, ROR4 = 0x124, ROR8 = 0x128, SHL4 = 0x104;
__device__ __forceinline__ float xrow_sum(float x) {
  auto s = __builtin_amdgcn_permlane16_swap(__float_as_uint(x), __float_as_uint(x), false, false);
  x = __uint_as_float(s[0]) + __uint_as_float(s[1]);
  auto t = __builtin_amdgcn_permlane32_swap(__float_as_uint(x), __float_as_uint(x), false, false);
  return __uint_as_float(t[0]) + __uint_as_float(t[1]);
}
__device__ __forceinline__ float xrow_max(float x) {
  auto s = __builtin_amdgcn_permlane16_swap(__float_as_uint(x), __float_as_uint(x), false, false);
  x = fmaxf(__uint_as_float(s[0]), __uint_as_float(s[1]));
  auto t = __builtin_amdgcn_permlane32_swap(__float_as_uint(x), __float_as_uint(x), false, false);
  return fmaxf(__uint_as_float(t[0]), __uint_as_float(t[1]));
}
__device__ __forceinline__ float x32_sum(float x) {
  auto t = __builtin_amdgcn_permlane32_swap(__float_as_uint(x), __float_as_uint(x), false, false);
  return __uint_as_float(t[0]) + __uint_as_float(t[1]);
}
__device__ __forceinline__ rsrc_t mkrsrc(const void* p, unsigned bytes) {
  return __builtin_amdgcn_make_buffer_rsrc((void*)p, 0, bytes, 0x00020000);
}
template <int AUX> __device__ __forceinline__ v4u ld128(rsrc_t rs, int voff, int soff) {
  return __builtin_amdgcn_raw_buffer_load_b128(rs, voff, soff, AUX);
}
__device__ __forceinline__ unsigned umax3(unsigned a, unsigned b, unsigned c) { return max(max(a, b), c); }
__device__ __forceinline__ unsigned vmax4(v4u x, unsigned m) { return umax3(x.x, x.y, umax3(x.z, x.w, m)); }

#define NPROBE 4
#define DLY 11
__device__ __forceinline__ unsigned long long now() { return __builtin_amdgcn_s_memrealtime(); }
__device__ __forceinline__ void hold(unsigned long long t0, int ticks) {
  if (ticks > 0) while ((long long)(now() - t0) < ticks) __builtin_amdgcn_s_sleep(1);
}
#define T_NEAR 1070
#define T_X1_H 70
#define T_WO_X2 265
#define T_GU_X2 140
#define T_Q_O 215
#define T_Q_X3 330
#define T_O_Q 380
#define SETTLE() __builtin_amdgcn_s_sleep(DLY)
__device__ __forceinline__ int xcc_id() { return __builtin_amdgcn_s_getreg(20 | (3 << 11)) & 7; }
__device__ __forceinline__ void near_probe(rsrc_t rws, int slot, unsigned tag, int l) {
  unsigned best0 = ~0u, best1 = ~0u;
#pragma unroll 1
  for (int i = 0; i < NPROBE; ++i) {
    unsigned long long t0, t1, t2;
    const int o0 = l == 0 ? dup(OFF_PROBE + slot * 128, 0) : OOB, o1 = l == 0 ? dup(OFF_PROBE + slot * 128, 1) : OOB;
    asm volatile("s_waitcnt vmcnt(0)\n\ts_memtime %0\n\ts_waitcnt lgkmcnt(0)\n\tbuffer_store_dword %3, %4, %6, 0 offen sc1\n\ts_waitcnt vmcnt(0)\n\t"
                 "s_memtime %1\n\ts_waitcnt lgkmcnt(0)\n\tbuffer_store_dword %3, %5, %6, 0 offen sc1\n\ts_waitcnt vmcnt(0)\n\ts_memtime %2\n\ts_waitcnt lgkmcnt(0)"
                 : "=&s"(t0), "=&s"(t1), "=&s"(t2) : "v"(i), "v"(o0), "v"(o1), "s"(rws) : "memory");
    best0 = min(best0, (unsigned)(t1 - t0)); best1 = min(best1, (unsigned)(t2 - t1));
  }
  if (l == 0) __builtin_amdgcn_raw_buffer_store_b32((tag << 1) | (best1 < best0 ? 1u : 0u), rws, OFF_NEAR + xcc_id() * 4, 0, AUX_SC1);
}
__device__ __forceinline__ int near_fetch(rsrc_t rws, unsigned tag) {
  const unsigned v = __builtin_amdgcn_readfirstlane(__builtin_amdgcn_raw_buffer_load_b32(rws, OFF_NEAR + xcc_id() * 4, 0, AUX_SC1));
  return (v >> 1) == (tag & 0x7FFFFFFFu) ? (int)(v & 1u) : 0;
}
template <int N> __device__ __forceinline__ void gather(v4u (&x)[N], rsrc_t rs, const int (&off_)[N], bool& dead, int nc) {
  int off[N];
#pragma unroll
  for (int i = 0; i < N; ++i) off[i] = dup(off_[i], nc);
  for (int spin = 0;; ++spin) {
    asm volatile("" ::: "memory");
#pragma unroll
    for (int i = 0; i < N; ++i) x[i] = ld128<AUX_SC1>(rs, off[i], 0);
    unsigned m = 0u;
#pragma unroll
    for (int i = 0; i < N; ++i) m = vmax4(x[i], m);
    if (!__any(m == SENT)) break;
    if (dead || spin > SPIN_CAP) { dead = true; break; }
  }
}
__device__ __forceinline__ unsigned long long epoch_load(rsrc_t rws) {
  const v2u e = __builtin_amdgcn_raw_buffer_load_b64(rws, OFF_EP, 0, AUX_SC1);
  return ((unsigned long long)(unsigned)__builtin_amdgcn_readfirstlane(e.y) << 32 | (unsigned)__builtin_amdgcn_readfirstlane(e.x)) + 1ull;
}
__device__ __forceinline__ void clear16(rsrc_t rws, int off) {
  __builtin_amdgcn_raw_buffer_store_b128(v4u{SENT, SENT, SENT, SENT}, rws, dup(off, 0), 0, AUX_SC1);
  __builtin_amdgcn_raw_buffer_store_b128(v4u{SENT, SENT, SENT, SENT}, rws, dup(off, 1), 0, AUX_SC1);
}
__device__ __forceinline__ void flag_post(rsrc_t rws, int cu, unsigned long long tag, int l) {
  asm volatile("s_waitcnt vmcnt(0)" ::: "memory");
  if (l == 0) __builtin_amdgcn_raw_buffer_store_b64(v2u{(unsigned)tag, (unsigned)(tag >> 32)}, rws, OFF_FLAG + cu * 8, 0, AUX_SC1);
}
__device__ __forceinline__ void flags_wait(rsrc_t rws, unsigned long long tag, int l, bool& dead) {
  const unsigned lo = (unsigned)tag, hi = (unsigned)(tag >> 32);
  for (int spin = 0;; ++spin) {
    asm volatile("" ::: "memory");
    const v4u a = ld128<AUX_SC1>(rws, OFF_FLAG + l * 32, 0), b = ld128<AUX_SC1>(rws, OFF_FLAG + l * 32 + 16, 0);
    const bool ok = (a.x == lo) & (a.y == hi) & (a.z == lo) & (a.w == hi) & (b.x == lo) & (b.y == hi) & (b.z == lo) & (b.w == hi);
    if (__all(ok)) break;
    if (dead || spin > SPIN_CAP) { dead = true; break; }
    __builtin_amdgcn_s_sleep(4);
  }
}
__device__ __forceinline__ v4u xpose16(v4u raw, v4u sel1, v4u sel2) {
  const f4 z = {0.f, 0.f, 0.f, 0.f};
  const f4 d1 = mfma(raw, sel1, z), d2 = mfma(raw, sel2, z);
  return v4u{hi16pack(d1[1], d1[0]), hi16pack(d1[3], d1[2]), hi16pack(d2[1], d2[0]), hi16pack(d2[3], d2[2])};
}
__device__ __forceinline__ void xpose32(v4u& a, v4u& b, v4u sel1, v4u sel2) {
  const f4 z = {0.f, 0.f, 0.f, 0.f};
  const f4 a1 = mfma(a, sel1, z), b1 = mfma(b, sel1, z), a2 = mfma(a, sel2, z), b2 = mfma(b, sel2, z);
  a = v4u{hi16pack(a1[1], a1[0]), hi16pack(a1[3], a1[2]), hi16pack(b1[1], b1[0]), hi16pack(b1[3], b1[2])};
  b = v4u{hi16pack(a2[1], a2[0]), hi16pack(a2[3], a2[2]), hi16pack(b2[1], b2[0]), hi16pack(b2[3], b2[2])};
}

#define ASM_LD(dst, voff, rs, soff, MODS) \
  asm volatile("buffer_load_dwordx4 %0, %1, %2, %3 offen " MODS : "=v"(dst) : "v"(voff), "s"(rs), "s"(soff) : "memory")
#define ASM_DMA(voff, rs, soff, ldsa) \
  asm volatile("s_mov_b32 m0, %3\n\ts_nop 0\n\tbuffer_load_dwordx4 %0, %1, %2 offen nt lds" ::"v"(voff), "s"(rs), "s"(soff), "s"(ldsa) : "memory")
#define WAIT_VM(n) asm volatile("s_waitcnt vmcnt(" #n ")" ::: "memory")
#define PIN8(a, o) asm volatile("" : "+v"(a[o]), "+v"(a[o + 1]), "+v"(a[o + 2]), "+v"(a[o + 3]), "+v"(a[o + 4]), "+v"(a[o + 5]), "+v"(a[o + 6]), "+v"(a[o + 7]))

__device__ __forceinline__ v2u tr_read(LAS unsigned char* p) {
  s4 t = __builtin_amdgcn_ds_read_tr16_b64_v4i16((LAS s4*)p);
  return __builtin_bit_cast(v2u, t);
}

__device__ __forceinline__ void attn_math(const int PASS, LAS unsigned char* L, const v4u (&kf)[32], const v4u (&vf)[16], const v4u (&qf)[4],
                                          rsrc_t rws, int b, int h, int w, int l_) {
  int l = l_; asm volatile("" : "+v"(l));
  const int g = l >> 4;
  const f4 z = {0.f, 0.f, 0.f, 0.f};
  constexpr float CS = 0.08838834764831845f * 1.4426950408889634f;
  float s0 = 0.f, s1 = 0.f;
#pragma unroll
  for (int t = 0; t < 8; t += 2) {
    f4 d0 = z, d1 = z;
#pragma unroll
    for (int s = 0; s < 4; ++s) { d0 = mfma(qf[s], kf[t * 4 + s], d0); d1 = mfma(qf[s], kf[t * 4 + 4 + s], d1); }
    if (t < 4) { s0 = (g == t) ? d0[0] : s0; s0 = (g == t + 1) ? d1[0] : s0; }
    else { s1 = (g == t - 4) ? d0[0] : s1; s1 = (g == t - 3) ? d1[0] : s1; }
  }
  float m = fmaxf(s0, s1);
  m = fmaxf(m, dpp<XOR1>(m)); m = fmaxf(m, dpp<XOR2>(m)); m = fmaxf(m, dpp<ROR4>(m)); m = fmaxf(m, dpp<ROR8>(m));
  m = xrow_max(m);
  const __bf16 p0 = (__bf16)__builtin_amdgcn_exp2f((s0 - m) * CS), p1 = (__bf16)__builtin_amdgcn_exp2f((s1 - m) * CS);
  LAS __bf16* pw = (LAS __bf16*)(L + L_P + w * 256);
  pw[l] = p0; pw[64 + l] = p1;
  v4u pf[4];
#pragma unroll
  for (int s = 0; s < 4; ++s) pf[s] = *(LAS v4u*)(L + L_P + w * 256 + s * 64 + g * 16);
  float ls = (float)p0 + (float)p1;
  ls += dpp<XOR1>(ls); ls += dpp<XOR2>(ls); ls += dpp<ROR4>(ls); ls += dpp<ROR8>(ls);
  ls = xrow_sum(ls);
  float oa0 = 0.f, oa1 = 0.f;
#pragma unroll
  for (int n = 0; n < 8; n += 2) {
    f4 d0 = mfma(pf[0], vf[n], z), d1 = mfma(pf[0], vf[n + 1], z);
    d0 = mfma(pf[1], vf[8 + n], d0); d1 = mfma(pf[1], vf[9 + n], d1);
    if (n < 4) { oa0 = (g == n) ? d0[0] : oa0; oa0 = (g == n + 1) ? d1[0] : oa0; }
    else { oa1 = (g == n - 4) ? d0[0] : oa1; oa1 = (g == n - 3) ? d1[0] : oa1; }
  }
  const int q4 = (l >> 2) & 3;
  const int trb = L_V + w * 16384 + (8 * g + q4) * 256 + ((q4 | ((g & 1) << 2)) << 5) + (l & 3) * 8;
  float ob0 = 0.f, ob1 = 0.f;
  if (PASS == 0) {
#pragma unroll
    for (int hb = 0; hb < 2; ++hb) {
      if (hb == 0) WAIT_VM(8); else WAIT_VM(0);
      v2u t0[4], t1[4];
      float pc0 = 0.f, pc1 = 0.f;
#define TR_LOAD(n) do { LAS unsigned char* a_ = L + (trb ^ ((n) << 5)) + hb * 8192; t0[(n) & 3] = tr_read(a_); t1[(n) & 3] = tr_read(a_ + 1024); } while (0)
      TR_LOAD(0); TR_LOAD(1);
#pragma unroll
      for (int n = 0; n < 8; n += 2) {
        if (n < 6) { TR_LOAD(n + 2); TR_LOAD(n + 3); }
        __builtin_amdgcn_sched_barrier(0);
        const f4 d0 = mfma(pf[2 + hb], v4u{t0[n & 3].x, t0[n & 3].y, t1[n & 3].x, t1[n & 3].y}, z);
        const f4 d1 = mfma(pf[2 + hb], v4u{t0[(n + 1) & 3].x, t0[(n + 1) & 3].y, t1[(n + 1) & 3].x, t1[(n + 1) & 3].y}, z);
        if (n < 4) { pc0 = (g == n) ? d0[0] : pc0; pc0 = (g == n + 1) ? d1[0] : pc0; }
        else { pc1 = (g == n - 4) ? d0[0] : pc1; pc1 = (g == n - 3) ? d1[0] : pc1; }
      }
      ob0 += pc0; ob1 += pc1;
    }
  } else {
    v2u t0[2], t1[2], t2[2], t3[2];
#define TR_LOAD2(n) do { LAS unsigned char* a_ = L + (trb ^ ((n) << 5)); \
      t0[(n) & 1] = tr_read(a_); t1[(n) & 1] = tr_read(a_ + 1024); t2[(n) & 1] = tr_read(a_ + 8192); t3[(n) & 1] = tr_read(a_ + 8192 + 1024); } while (0)
    TR_LOAD2(0);
#pragma unroll
    for (int n = 0; n < 8; ++n) {
      if (n < 7) TR_LOAD2(n + 1);
      __builtin_amdgcn_sched_barrier(0);
      f4 d = mfma(pf[2], v4u{t0[n & 1].x, t0[n & 1].y, t1[n & 1].x, t1[n & 1].y}, z);
      d = mfma(pf[3], v4u{t2[n & 1].x, t2[n & 1].y, t3[n & 1].x, t3[n & 1].y}, d);
      if (n < 4) ob0 = (g == n) ? d[0] : ob0; else ob1 = (g == n - 4) ? d[0] : ob1;
    }
  }
  LAS float* ow = (LAS float*)(L + L_OW);
  ow[w * 128 + l] = oa0 + ob0; ow[w * 128 + 64 + l] = oa1 + ob1;
  if (l == 0) *(LAS f2*)(L + L_ML + w * 8) = f2{m, ls};
  __syncthreads();
  if (w == 0) {
    f4 ml[4];
    f2 ov[8];
#pragma unroll
    for (int i = 0; i < 4; ++i) ml[i] = *(LAS f4*)(L + L_ML + i * 16);
#pragma unroll
    for (int i = 0; i < 8; ++i) ov[i] = *(LAS f2*)(L + L_OW + i * 512 + l * 8);
    float M = fmaxf(fmaxf(fmaxf(ml[0][0], ml[0][2]), fmaxf(ml[1][0], ml[1][2])), fmaxf(fmaxf(ml[2][0], ml[2][2]), fmaxf(ml[3][0], ml[3][2])));
    float Ls = 0.f, o0 = 0.f, o1 = 0.f;
#pragma unroll
    for (int i = 0; i < 8; ++i) {
      const float f = __builtin_amdgcn_exp2f((ml[i >> 1][(i & 1) * 2] - M) * CS);
      Ls = fmaf(f, ml[i >> 1][(i & 1) * 2 + 1], Ls); o0 = fmaf(f, ov[i].x, o0); o1 = fmaf(f, ov[i].y, o1);
    }
    const float inv = __builtin_amdgcn_rcpf(Ls);
    const unsigned ob = pack_bf16(o0 * inv, o1 * inv);
    const int oo = OFF_O + PASS * SZ_O + (h * 4 + b) * 256 + l * 4;
    __builtin_amdgcn_raw_buffer_store_b32(ob, rws, dup(oo, 0), 0, AUX_SC1);
    __builtin_amdgcn_raw_buffer_store_b32(ob, rws, dup(oo, 1), 0, AUX_SC1);
  }
}

__device__ __forceinline__ void attn_role(LAS unsigned char* L, const u16* K, const u16* V, rsrc_t rws, int cu, int xc, int y, int w, int l) {
  const int h = 4 * xc + (y >> 2), b = y & 3, g = l >> 4, r = l & 15;
  const size_t kv = ((size_t)(b * NH + h) * SK + (SK - WIN) + 128 * w) * DH;
  const rsrc_t rK = mkrsrc(K + kv, 32768), rV = mkrsrc(V + kv, 32768);
  bool dead = false;
  v4u kf[32], vf[16], qf[4];
  const int ldsV = L_V + w * 16384;
  const int ks0 = g * 256 + ((r ^ g) << 4);
#define KX(i) (((i) & 3) << 6)
  const int vs0 = g * 256 + (((r >> 1) ^ g) << 5) + (l & 1) * 16, vs1 = g * 256 + (((r >> 1) ^ (g | 4)) << 5) + (l & 1) * 16;
#define K_PIECE(n) _Pragma("unroll") for (int i = 0; i < 8; ++i) \
    ASM_DMA(ks0 ^ KX(i), rK, (n) * 8192 + i * 1024, ldsV + ((n) & 1) * 8192 + i * 1024)
#define V_PIECE(n, buf) _Pragma("unroll") for (int i = 0; i < 8; ++i) \
    ASM_DMA((i & 2) ? vs1 : vs0, rV, (n) * 8192 + i * 1024, ldsV + (buf) * 8192 + i * 1024)
#define K_READ(n) do { _Pragma("unroll") for (int t = 0; t < 2; ++t) _Pragma("unroll") for (int s = 0; s < 4; ++s) \
      kf[((n) * 2 + t) * 4 + s] = *(LAS v4u*)(L + ldsV + ((n) & 1) * 8192 + (16 * t + r) * 256 + (((4 * s + g) ^ r) << 4)); \
    asm volatile("s_waitcnt lgkmcnt(0)" ::: "memory"); PIN8(kf, (n) * 8); } while (0)
  const int trb = ldsV + (8 * g + ((l >> 2) & 3)) * 256 + ((((l >> 2) & 3) | ((g & 1) << 2)) << 5) + (l & 3) * 8;
#define V_READ(n) do { _Pragma("unroll") for (int c8 = 0; c8 < 8; ++c8) { \
      LAS unsigned char* a = L + (trb ^ (c8 << 5)) + (n) * 8192; \
      const v2u t0 = tr_read(a), t1 = tr_read(a + 1024); \
      vf[(n) * 8 + c8] = v4u{t0.x, t0.y, t1.x, t1.y}; } \
    asm volatile("s_waitcnt lgkmcnt(0)" ::: "memory"); PIN8(vf, (n) * 8); } while (0)
  K_PIECE(0);
  if (threadIdx.x == 0) *(volatile LAS unsigned*)(L + L_QF) = 0u;
  asm volatile("s_waitcnt lgkmcnt(0)\n\ts_barrier" ::: "memory");
  v2u ep = {0u, 0u};
  unsigned long long tag = 0;
  {
    const v4u sent = {SENT, SENT, SENT, SENT};
    const int co = dup(OFF_O + ((l >> 4) & 1) * SZ_O + (h * 4 + b) * 256 + r * 16, l >> 5);
    asm volatile("s_cmp_eq_u32 %5, 0\n\ts_cbranch_scc0 1f\n\tbuffer_load_dwordx2 %0, %1, %4, 0 offen sc1\n\t"
                 "buffer_store_dwordx4 %2, %3, %4, 0 offen sc1\n\ts_nop 1\n1:"
                 : "+v"(ep) : "v"(OFF_EP), "v"(sent), "v"(co), "s"(rws), "s"(w) : "memory", "scc");
  }
  K_PIECE(1);
  WAIT_VM(8);
  asm volatile("" : "+v"(ep));
  if (w == 0) {
    tag = ((unsigned long long)(unsigned)__builtin_amdgcn_readfirstlane(ep.y) << 32 | (unsigned)__builtin_amdgcn_readfirstlane(ep.x)) + 1ull;
    const v2u tg = {(unsigned)tag, (unsigned)(tag >> 32)};
    asm volatile("buffer_store_dwordx2 %0, %1, %2, 0 offen sc1\n\ts_nop 0" ::"v"(tg), "v"(l == 0 ? OFF_FLAG + cu * 8 : OOB), "s"(rws) : "memory");
  }
  K_READ(0); K_PIECE(2);
  WAIT_VM(8); K_READ(1); K_PIECE(3);
  WAIT_VM(8); K_READ(2); V_PIECE(0, 0);
  WAIT_VM(8); K_READ(3); V_PIECE(1, 1);
  if (w == 0) {
    flags_wait(rws, tag, l, dead);
    v4u q1[1];
    const int off[1] = {l < 16 ? OFF_Q + (h * 16 + b) * 64 + (l >> 2) * 256 + (l & 3) * 16 : OOB};
    gather<1>(q1, rws, off, dead, 0);
    if (l < 16) *(LAS v4u*)(L + L_Q + l * 16) = q1[0];
    asm volatile("s_waitcnt lgkmcnt(0)" ::: "memory");
    if (l == 0) *(volatile LAS unsigned*)(L + L_QF) = 1u;
  }
  WAIT_VM(8); V_READ(0); V_PIECE(2, 0);
  WAIT_VM(8); V_READ(1); V_PIECE(3, 1);
#pragma unroll 1
  for (int pass = 0; pass < 2; ++pass) {
    if (pass == 0) {
      for (int spin = 0; *(volatile LAS unsigned*)(L + L_QF) == 0u; ++spin) {
        if (spin > (SPIN_CAP << 4)) { dead = true; break; }
        __builtin_amdgcn_s_sleep(1);
      }
#pragma unroll
      for (int s = 0; s < 4; ++s) qf[s] = *(LAS v4u*)(L + L_Q + s * 64 + g * 16);
      asm volatile("" ::: "memory");
    } else {
      const unsigned long long tr = now();
      if (w == 0) { const int n_ = near_fetch(rws, (unsigned)tag); if (l == 0) *(volatile LAS unsigned*)(L + L_QF + 4) = n_; }
      __syncthreads();
      const int nc = __builtin_amdgcn_readfirstlane(*(volatile LAS unsigned*)(L + L_QF + 4));
      hold(tr, T_O_Q);
      int lo = l; asm volatile("" : "+v"(lo));
      const int qo = OFF_Q + SZ_Q + (h * 16 + b) * 64 + (lo >> 4) * 16;
      const int off[4] = {qo, qo + 256, qo + 512, qo + 768};
      gather<4>(qf, rws, off, dead, nc);
    }
    attn_math(pass, L, kf, vf, qf, rws, b, h, w, l);
  }
}

__device__ __forceinline__ f4 red_sum(LAS unsigned char* L, int row, int l) {
  const int hf = l >> 5, cg = l & 7;
  f4 s = *(LAS f4*)(L + L_RED + (hf * 4) * RED_W + row * 144 + cg * 16);
#pragma unroll
  for (int i = 1; i < 4; ++i) s += *(LAS f4*)(L + L_RED + (hf * 4 + i) * RED_W + row * 144 + cg * 16);
  return f4{x32_sum(s[0]), x32_sum(s[1]), x32_sum(s[2]), x32_sum(s[3])};
}
__device__ __forceinline__ f2 red_stat(LAS unsigned char* L, int row, int l) {
  const int hf = l >> 5;
  f2 s = *(LAS f2*)(L + L_RED + (hf * 4) * RED_W + row * 144 + 128);
#pragma unroll
  for (int i = 1; i < 4; ++i) s += *(LAS f2*)(L + L_RED + (hf * 4 + i) * RED_W + row * 144 + 128);
  return f2{x32_sum(s[0]), x32_sum(s[1])};
}

__device__ __forceinline__ f4 red_sum_gu(LAS unsigned char* L, int row, int l) {
  const int m = l >> 5, cg = l & 7;
  f4 s = *(LAS f4*)(L + L_RED + m * RED_W + row * 144 + cg * 16);
#pragma unroll
  for (int i = 1; i < 4; ++i) s += *(LAS f4*)(L + L_RED + (2 * i + m) * RED_W + row * 144 + cg * 16);
  return s;
}
struct WArgs { const u16 *x, *wq, *wo, *wg, *wu, *wd; u16* out; };

template <int PASS>
__device__ __forceinline__ void q_phase(LAS unsigned char* L, const v4u (&xf)[3], const v4u (&wqf)[6], f4 csq, rsrc_t rws, int tq, int w, int l) {
  const int g = l >> 4, c = l & 15;
  const f4 z = {0.f, 0.f, 0.f, 0.f};
  const v4u ones = {0x3F803F80u, 0x3F803F80u, 0x3F803F80u, 0x3F803F80u};
  f4 d0 = z, d1 = z, ds = z;
  float ss = 0.f;
#pragma unroll
  for (int i = 0; i < 3; ++i) {
    d0 = mfma(wqf[2 * i], xf[i], d0); d1 = mfma(wqf[2 * i + 1], xf[i], d1); ds = mfma(ones, xf[i], ds);
    ss = dot2(xf[i].x, xf[i].x, ss); ss = dot2(xf[i].y, xf[i].y, ss); ss = dot2(xf[i].z, xf[i].z, ss); ss = dot2(xf[i].w, xf[i].w, ss);
  }
  ss = xrow_sum(ss);
  LAS unsigned char* R = L + L_RED + w * RED_W + c * 144;
  if (c < 4) {
    *(LAS f4*)(R + g * 16) = d0; *(LAS f4*)(R + 64 + g * 16) = d1;
    if (g == 0) *(LAS f2*)(R + 128) = f2{ds[0], ss};
  }
  __syncthreads();
  if (w == 0) {
    const int b = (l >> 3) & 3, cg = l & 7;
    const f4 p = red_sum(L, b, l);
    const f2 s = red_stat(L, b, l);
    const float mu = s[0] * (1.0f / D), var = fmaxf(s[1] * (1.0f / D) - mu * mu, 0.f), rs = __builtin_amdgcn_rsqf(var + 1e-5f);
    {
      const v2u o = {pack_bf16((p[0] - mu * csq[0]) * rs, (p[1] - mu * csq[1]) * rs), pack_bf16((p[2] - mu * csq[2]) * rs, (p[3] - mu * csq[3]) * rs)};
      __builtin_amdgcn_raw_buffer_store_b64(o, rws, dup(OFF_Q + PASS * SZ_Q + tq * 256 + b * 64 + cg * 8, l >> 5), 0, AUX_SC1);
    }
  }
  __syncthreads();
}

__device__ __forceinline__ void q_second(LAS unsigned char* L, const v4u (&wqf)[6], f4 csq, rsrc_t rws, int tq, int w, int l, bool& dead, int nc) {
  const int g = l >> 4, c = l & 15;
  v4u xf[3];
  int off[3];
#pragma unroll
  for (int i = 0; i < 3; ++i) {
    const int s = w + 8 * i;
    off[i] = (c < 4 && s < 20) ? OFF_X2 + ((2 * s + (g >> 1)) * 4 + c) * 32 + (g & 1) * 16 : OOB;
  }
  gather<3>(xf, rws, off, dead, nc);
  q_phase<1>(L, xf, wqf, csq, rws, tq, w, l);
}

__device__ __forceinline__ void weight_role(LAS unsigned char* L, const WArgs& a, rsrc_t rws, int cu, int xc, int u, int w, int l) {
  const int g = l >> 4, r = l & 15, c = r;
  const f4 z = {0.f, 0.f, 0.f, 0.f};
  const v4u ones = {0x3F803F80u, 0x3F803F80u, 0x3F803F80u, 0x3F803F80u};
  v4u sel1, sel2;
  {
    const int j = r - 8 * (g & 1);
    const unsigned one = (j & 1) ? 0x3F800000u : 0x00003F80u;
    const bool in = (unsigned)j < 8u;
    v4u s = {(in && (j >> 1) == 0) ? one : 0u, (in && (j >> 1) == 1) ? one : 0u, (in && (j >> 1) == 2) ? one : 0u, (in && (j >> 1) == 3) ? one : 0u};
    const v4u zz = {0u, 0u, 0u, 0u};
    sel1 = g < 2 ? s : zz; sel2 = g < 2 ? zz : s;
  }
  const int tq = xc * 16 + u;
  const bool has_wo = u < 10, has_gu = u < (xc < 3 ? 8 : 6), has_dn = u >= 10 && u < 15;
  const int wo_id = xc * 10 + u, kq = wo_id / 20, nt32 = wo_id % 20;
  const int t32 = xc < 3 ? xc * 8 + u : 24 + (xc - 3) * 6 + u;
  const int td = xc * 5 + (u - 10);
  const int rho16 = 8 * (r >> 2) + (r & 3);
  const int rho32 = rho16 + 4 * (g >> 1);

  bool dead = false;
  const unsigned long long tstart = now();
  v2u ep = {0u, 0u};
  if (w == 0) {
    ep = __builtin_amdgcn_raw_buffer_load_b64(rws, OFF_EP, 0, AUX_SC1);
    const int hf = l >> 5, k = l & 31;
    clear16(rws, k < 16 ? OFF_Q + hf * SZ_Q + tq * 256 + k * 16 : has_wo ? OFF_X1 + hf * SZ_X1 + (kq * 40 + 2 * nt32) * 128 + (k - 16) * 16 : OOB);
    clear16(rws, (k < 16 && has_gu) ? OFF_H + hf * SZ_H + t32 * 256 + k * 16 : (k < 8 && hf == 0 && has_dn) ? OFF_X2 + td * 128 + k * 16 : OOB);
  }
  v4u wqf[6];
  unsigned long long tag = 0;
  f4 csq;
  {
    v4u xf[3];
    const rsrc_t rx = mkrsrc(a.x, NB * D * 2), rq = mkrsrc(a.wq, D * HD * 2);
#pragma unroll
    for (int i = 0; i < 3; ++i) {
      const int s = w + 8 * i;
      xf[i] = ld128<0>(rx, (c < 4 && s < 20) ? c * 1280 + s * 64 + g * 16 : OOB, 0);
    }
#pragma unroll
    for (int i = 0; i < 3; ++i) {
      const int s = w + 8 * i;
      const int vo = s < 20 ? (32 * s + rho16) * 8192 + tq * 64 + g * 16 : OOB;
      wqf[2 * i] = ld128<AUX_NT>(rq, vo, 0); wqf[2 * i + 1] = ld128<AUX_NT>(rq, vo, 4 * 8192);
    }
    __builtin_amdgcn_sched_barrier(0);
    if (w == 0) {
      asm volatile("s_waitcnt vmcnt(9)" : "+v"(ep)::"memory");
      tag = ((unsigned long long)(unsigned)__builtin_amdgcn_readfirstlane(ep.y) << 32 | (unsigned)__builtin_amdgcn_readfirstlane(ep.x)) + 1ull;
      if (l == 0) __builtin_amdgcn_raw_buffer_store_b64(v2u{(unsigned)tag, (unsigned)(tag >> 32)}, rws, OFF_FLAG + cu * 8, 0, AUX_SC1);
    }
#pragma unroll
    for (int i = 0; i < 3; ++i) xpose32(wqf[2 * i], wqf[2 * i + 1], sel1, sel2);
    f4 c0 = z, c1 = z;
#pragma unroll
    for (int i = 0; i < 3; ++i) { c0 = mfma(wqf[2 * i], ones, c0); c1 = mfma(wqf[2 * i + 1], ones, c1); }
    LAS unsigned char* R4 = L + L_RED + w * RED_W + 4 * 144;
    if (c == 0) { *(LAS f4*)(R4 + g * 16) = c0; *(LAS f4*)(R4 + 64 + g * 16) = c1; }
    __syncthreads();
    csq = red_sum(L, 4, l);
    q_phase<0>(L, xf, wqf, csq, rws, tq, w, l);
  }
  const int fb = (l >> 3) & 3, fcg = l & 7;
  LAS unsigned char* R = L + L_RED + w * RED_W + c * 144;

  if (has_wo) {
    v4u wof[8], guf[10];
    const int gm = (w ^ (w >> 2)) & 1, gq = w >> 1;
    LAS unsigned char* Rg = L + L_RED + (2 * gq + gm) * RED_W + c * 144;
    {
      const rsrc_t ro = mkrsrc(a.wo, D * HD * 2), rgu = mkrsrc(gm ? a.wu : a.wg, D * FF * 2);
      const int vo = ((8 * kq + w) * 128 + rho16) * 1280 + nt32 * 64 + g * 16;
#pragma unroll
      for (int s = 0; s < 4; ++s) { wof[2 * s] = ld128<AUX_NT>(ro, vo, s * 32 * 1280); wof[2 * s + 1] = ld128<AUX_NT>(ro, vo, (s * 32 + 4) * 1280); }
      const int vg = has_gu ? (160 * gq + rho16) * 3456 + t32 * 64 + g * 16 : OOB;
#pragma unroll
      for (int i = 0; i < 5; ++i) { guf[2 * i] = ld128<AUX_NT>(rgu, vg, i * 32 * 3456); guf[2 * i + 1] = ld128<AUX_NT>(rgu, vg, (i * 32 + 4) * 3456); }
      __builtin_amdgcn_sched_barrier(0);
    }
#pragma unroll
    for (int s = 0; s < 4; ++s) xpose32(wof[2 * s], wof[2 * s + 1], sel1, sel2);
#pragma unroll
    for (int i = 0; i < 5; ++i) xpose32(guf[2 * i], guf[2 * i + 1], sel1, sel2);
    f4 csg;
    {
      f4 c0 = z, c1 = z;
#pragma unroll
      for (int i = 0; i < 5; ++i) { c0 = mfma(guf[2 * i], ones, c0); c1 = mfma(guf[2 * i + 1], ones, c1); }
      LAS unsigned char* R4 = L + L_RED + (2 * gq + gm) * RED_W + 4 * 144;
      if (c == 0) { *(LAS f4*)(R4 + g * 16) = c0; *(LAS f4*)(R4 + 64 + g * 16) = c1; }
      if (w == 0) flags_wait(rws, tag, l, dead);
      __syncthreads();
      csg = red_sum_gu(L, 4, l);
    }
    hold(tstart, T_NEAR);
    if (w == 0) { const int n_ = near_fetch(rws, (unsigned)tag); if (l == 0) *(volatile LAS unsigned*)(L + L_NEAR) = n_; }
    __syncthreads();
    const int nc = __builtin_amdgcn_readfirstlane(*(volatile LAS unsigned*)(L + L_NEAR));
    hold(tstart, HOLD_T);
#pragma unroll 1
    for (int pass = 0; pass < 2; ++pass) {
      {
        v4u of[4];
        int lo = l; asm volatile("" : "+v"(lo));
        const int o0 = (lo & 15) < 4 ? OFF_O + pass * SZ_O + ((8 * kq + w) * 4 + (lo & 15)) * 256 + (lo >> 4) * 16 : OOB;
        const int off[4] = {o0, o0 + 64, o0 + 128, o0 + 192};
        v2u res = {0u, 0u};
        if (w == 0 && kq == 0) {
          if (pass == 0) res = *(const v2u*)(a.x + fb * D + nt32 * 32 + fcg * 4);
          else res = __builtin_amdgcn_raw_buffer_load_b64(rws, dup(OFF_X2 + ((2 * nt32 + (fcg >> 2)) * 4 + fb) * 32 + (fcg & 3) * 8, nc), 0, AUX_SC1);
        }
        gather<4>(of, rws, off, dead, nc);
        f4 d0 = z, d1 = z;
#pragma unroll
        for (int s = 0; s < 4; ++s) { d0 = mfma(wof[2 * s], of[s], d0); d1 = mfma(wof[2 * s + 1], of[s], d1); }
        if (c < 4) { *(LAS f4*)(R + g * 16) = d0; *(LAS f4*)(R + 64 + g * 16) = d1; }
        __syncthreads();
        if (w == 0) {
          f4 p = red_sum(L, fb, l);
          p[0] += bflo(res.x); p[1] += bfhi(res.x); p[2] += bflo(res.y); p[3] += bfhi(res.y);
          {
            const v2u o = {pack_bf16(p[0], p[1]), pack_bf16(p[2], p[3])};
            __builtin_amdgcn_raw_buffer_store_b64(o, rws, dup(OFF_X1 + pass * SZ_X1 + ((kq * 40 + 2 * nt32 + (fcg >> 2)) * 4 + fb) * 32 + (fcg & 3) * 8, l >> 5), 0, AUX_SC1);
          }
        }
        __syncthreads();
      }
      if (has_gu) {
        v4u x1[5];
        int xo[5];
        int lo = l; asm volatile("" : "+v"(lo));
#pragma unroll
        for (int i = 0; i < 5; ++i)
          xo[i] = OFF_X1 + pass * SZ_X1 + ((((lo & 15) >> 2) * 40 + 2 * (5 * gq + i) + (lo >> 5)) * 4 + (lo & 3)) * 32 + ((lo >> 4) & 1) * 16;
        SETTLE();
        gather<5>(x1, rws, xo, dead, nc);
        f4 d0 = z, d1 = z, ds = z;
#pragma unroll
        for (int i = 0; i < 5; ++i) { d0 = mfma(guf[2 * i], x1[i], d0); d1 = mfma(guf[2 * i + 1], x1[i], d1); }
        if (gm == 0) {
#pragma unroll
          for (int i = 0; i < 5; ++i) ds = mfma(ones, x1[i], ds);
        }
#pragma unroll
        for (int i = 0; i < 4; ++i) {
          d0[i] += dpp<ROR4>(d0[i]); d0[i] += dpp<ROR8>(d0[i]);
          d1[i] += dpp<ROR4>(d1[i]); d1[i] += dpp<ROR8>(d1[i]);
        }
        float sx = ds[0]; sx += dpp<ROR4>(sx); sx += dpp<ROR8>(sx);
        if (c < 4) {
          *(LAS f4*)(Rg + g * 16) = d0; *(LAS f4*)(Rg + 64 + g * 16) = d1;
          if (g == 0) *(LAS float*)(Rg + 128) = sx;
        }
        __syncthreads();
        if (w == 0) {
          const f4 p = red_sum_gu(L, fb, l);
          float sm = 0.f;
#pragma unroll
          for (int i = 0; i < 4; ++i) sm += *(LAS float*)(L + L_RED + 2 * i * RED_W + fb * 144 + 128);
          const float mu = sm * (1.0f / D);
          float hv[4];
#pragma unroll
          for (int i = 0; i < 4; ++i) {
            const float v = p[i] - mu * csg[i];
            auto sw = __builtin_amdgcn_permlane32_swap(__float_as_uint(v), __float_as_uint(v), false, false);
            hv[i] = fmaxf(__uint_as_float(sw[0]), 0.f) * __uint_as_float(sw[1]);
          }
          {
            const v2u o = {pack_bf16(hv[0], hv[1]), pack_bf16(hv[2], hv[3])};
            __builtin_amdgcn_raw_buffer_store_b64(o, rws, dup(OFF_H + pass * SZ_H + ((2 * t32 + (fcg >> 2)) * 4 + fb) * 32 + (fcg & 3) * 8, l >> 5), 0, AUX_SC1);
          }
        }
        __syncthreads();
      }
      if (pass == 0) { hold(now(), has_gu ? T_GU_X2 : T_WO_X2); q_second(L, wqf, csq, rws, tq, w, l, dead, nc); hold(now(), T_Q_O); }
    }
  } else {
    v4u wdf[7];
    if (!has_dn && w == 0) { near_probe(rws, xc, (unsigned)tag, l); }
#define T_DN_LOAD 600
    hold(tstart, T_DN_LOAD);
    {
      const rsrc_t rd = mkrsrc(a.wd, D * FF * 2);
#pragma unroll
      for (int i = 0; i < 7; ++i) {
        const int s = w + 8 * i;
        wdf[i] = ld128<AUX_NT>(rd, (has_dn && s < 54) ? (32 * s + rho32) * 1280 + td * 32 + (g & 1) * 16 : OOB, 0);
      }
      __builtin_amdgcn_sched_barrier(0);
    }
#pragma unroll
    for (int i = 0; i < 7; ++i) wdf[i] = xpose16(wdf[i], sel1, sel2);
    if (w == 0) {
      flags_wait(rws, tag, l, dead);
      if (has_dn && td == 0 && l == 0) __builtin_amdgcn_raw_buffer_store_b64(v2u{(unsigned)tag, (unsigned)(tag >> 32)}, rws, OFF_EP, 0, AUX_SC1);
    }
    __syncthreads();
    hold(tstart, T_NEAR);
    if (w == 0) { const int n_ = near_fetch(rws, (unsigned)tag); if (l == 0) *(volatile LAS unsigned*)(L + L_NEAR) = n_; }
    __syncthreads();
    const int nc = __builtin_amdgcn_readfirstlane(*(volatile LAS unsigned*)(L + L_NEAR));
    hold(tstart, HOLD_T);
#pragma unroll 1
    for (int pass = 0; pass < 2; ++pass) {
      if (has_dn) {
        v4u x1[3];
        int xoff[3];
        int lo = l; asm volatile("" : "+v"(lo));
#pragma unroll
        for (int i = 0; i < 3; ++i) {
          const int s = w + 8 * i;
          xoff[i] = s < 20 ? OFF_X1 + pass * SZ_X1 + ((((lo & 15) >> 2) * 40 + 2 * s + (lo >> 5)) * 4 + (lo & 3)) * 32 + ((lo >> 4) & 1) * 16 : OOB;
        }
        gather<3>(x1, rws, xoff, dead, nc);
        const unsigned long long tx1 = now();
        f4 gram = z, ds = z;
#pragma unroll
        for (int i = 0; i < 3; ++i) { gram = mfma(x1[i], x1[i], gram); ds = mfma(ones, x1[i], ds); }
        float sq = (c & 3) == 0 ? gram[0] : (c & 3) == 1 ? gram[1] : (c & 3) == 2 ? gram[2] : gram[3];
        sq += dpp<ROR4>(sq); sq += dpp<ROR8>(sq); sq = xrow_sum(sq);
        float sx = ds[0]; sx += dpp<ROR4>(sx); sx += dpp<ROR8>(sx);
        if (c < 4 && g == 0) *(LAS f2*)(R + 128) = f2{sx, sq};
        __syncthreads();
        float rs2 = 0.f; f4 res = z;
        if (w == 0) {
          v2u rp[4];
#pragma unroll
          for (int k = 0; k < 4; ++k)
            rp[k] = __builtin_amdgcn_raw_buffer_load_b64(rws, dup(OFF_X1 + pass * SZ_X1 + ((k * 40 + td) * 4 + fb) * 32 + (fcg & 3) * 8, nc), 0, AUX_SC1);
          const f2 s = red_stat(L, fb, l);
          const float mu = s[0] * (1.0f / D), var = fmaxf(s[1] * (1.0f / D) - mu * mu, 0.f);
          rs2 = __builtin_amdgcn_rcpf(var + 1e-5f);
#pragma unroll
          for (int k = 0; k < 4; ++k) { res[0] += bflo(rp[k].x); res[1] += bfhi(rp[k].x); res[2] += bflo(rp[k].y); res[3] += bfhi(rp[k].y); }
        }
        v4u hf[7];
        int hoff[7];
#pragma unroll
        for (int i = 0; i < 7; ++i) {
          const int s = w + 8 * i;
          hoff[i] = ((lo & 15) < 4 && s < 54) ? OFF_H + pass * SZ_H + ((2 * s + (lo >> 5)) * 4 + (lo & 15)) * 32 + ((lo >> 4) & 1) * 16 : OOB;
        }
        hold(tx1, T_X1_H);
        gather<7>(hf, rws, hoff, dead, nc);
        f4 d = z;
#pragma unroll
        for (int i = 0; i < 7; ++i) d = mfma(wdf[i], hf[i], d);
        if (c < 4) { *(LAS f4*)(R + g * 16) = d; *(LAS f4*)(R + 64 + g * 16) = z; }
        __syncthreads();
        if (w == 0) {
          const f4 p = red_sum(L, fb, l);
          if (fcg < 4) {
            f4 o;
#pragma unroll
            for (int i = 0; i < 4; ++i) o[i] = res[i] + rs2 * p[i];
            if (dead) o = f4{__builtin_nanf(""), __builtin_nanf(""), __builtin_nanf(""), __builtin_nanf("")};
            const v2u ob = {pack_bf16(o[0], o[1]), pack_bf16(o[2], o[3])};
            if (pass == 0) __builtin_amdgcn_raw_buffer_store_b64(ob, rws, dup(OFF_X2 + (td * 4 + fb) * 32 + fcg * 8, l >> 5), 0, AUX_SC1);
            else __builtin_amdgcn_raw_buffer_store_b64(ob, mkrsrc(a.out, NB * D * 2), l < 32 ? (fb * D + td * 16 + fcg * 4) * 2 : OOB, 0, OUT_SC1);
          }
        }
        __syncthreads();
      }
      if (pass == 0) { if (has_dn) SETTLE(); q_second(L, wqf, csq, rws, tq, w, l, dead, nc); hold(now(), T_Q_X3); }
    }
  }
}

__global__ __launch_bounds__(512, 2) void mega_kernel(const u16* __restrict__ x, const u16* __restrict__ K, const u16* __restrict__ V,
                                                      const u16* __restrict__ wq, const u16* __restrict__ wo, const u16* __restrict__ wg,
                                                      const u16* __restrict__ wu, const u16* __restrict__ wd, u16* __restrict__ out,
                                                      unsigned char* __restrict__ ws) {
  asm volatile("" ::"s"(x), "s"(K), "s"(V), "s"(wq), "s"(wo), "s"(wg), "s"(wu), "s"(wd), "s"(out), "s"(ws));
  __shared__ __attribute__((aligned(16))) unsigned char lds[L_END];
  LAS unsigned char* L = (LAS unsigned char*)lds;
  const int cu = blockIdx.x, xc = cu & 7, y = cu >> 3;
  const int l = threadIdx.x & 63, w = __builtin_amdgcn_readfirstlane(threadIdx.x >> 6);
  const rsrc_t rws = mkrsrc(ws, WS_USED);
  if (y < 16) attn_role(L, K, V, rws, cu, xc, y, w, l);
  else {
    WArgs a{x, wq, wo, wg, wu, wd, out};
    weight_role(L, a, rws, cu, xc, y - 16, w, l);
  }
}
}

extern "C" void kernel_launch(void* const* d_in, const int* in_sizes, int n_in, void* d_out, int out_size, void* d_ws, size_t ws_size,
                              hipStream_t stream) {
  (void)in_sizes; (void)n_in; (void)out_size; (void)ws_size;
  mega_kernel<<<256, 512, 0, stream>>>((const u16*)d_in[0], (const u16*)d_in[1], (const u16*)d_in[2], (const u16*)d_in[3],
                                       (const u16*)d_in[4], (const u16*)d_in[6], (const u16*)d_in[7], (const u16*)d_in[8],
                                       (u16*)d_out, (unsigned char*)d_ws);
}
```

```cpp
#include <hip/hip_runtime.h>
#include <stdint.h>


namespace {
typedef unsigned short u16;
typedef unsigned v4u __attribute__((ext_vector_type(4)));
typedef unsigned v2u __attribute__((ext_vector_type(2)));
typedef float f4 __attribute__((ext_vector_type(4)));
typedef float f2 __attribute__((ext_vector_type(2)));
typedef __bf16 bf8 __attribute__((ext_vector_type(8)));
typedef __bf16 bf2 __attribute__((ext_vector_type(2)));
typedef short s4 __attribute__((ext_vector_type(4)));
typedef __amdgpu_buffer_rsrc_t rsrc_t;
#define LAS __attribute__((address_space(3)))

constexpr int NB = 4, D = 640, NH = 32, DH = 128, SK = 4096, WIN = 1024, FF = 1728, HD = 4096;
constexpr unsigned SENT = 0xFFFFFFFFu;
constexpr int OOB = 0x40000000;
constexpr int SZ_Q = 32 * 4 * 256, SZ_O = 32 * 4 * 256, SZ_X1 = 4 * 40 * 128, SZ_H = 108 * 128, SZ_X2 = 40 * 128;
constexpr int OFF_Q = 0, OFF_O = OFF_Q + 2 * SZ_Q, OFF_X1 = OFF_O + 2 * SZ_O, OFF_H = OFF_X1 + 2 * SZ_X1,
              OFF_X2 = OFF_H + 2 * SZ_H, OFF_PROBE = OFF_X2 + SZ_X2, DATA_END = OFF_PROBE + 8 * 128,
              OFF_FLAG = 2 * ((DATA_END + 8191) & ~8191), OFF_EP = OFF_FLAG + 2048, OFF_NEAR = OFF_EP + 128, WS_USED = OFF_NEAR + 128;
__device__ __forceinline__ int dup(int off, int copy) {
  return (int)((unsigned)off + ((unsigned)off & ~8191u) + ((unsigned)copy << 13));
}
constexpr int SPIN_CAP = 1 << 17;
constexpr int L_V = 0;
constexpr int L_P = 131072;
constexpr int L_OW = L_P + 2048;
constexpr int L_ML = L_OW + 4096;
constexpr int L_Q = L_ML + 64;
constexpr int L_QF = L_Q + 256;
constexpr int L_END = L_QF + 16;
constexpr int L_RED = 0;
constexpr int RED_W = 5 * 36 * 4;
constexpr int L_NEAR = 6144;

constexpr int AUX_NT = 2, AUX_SC1 = 16;
constexpr int HOLD_T = 1150;
constexpr int T_NEAR = 1070;
constexpr int T_DN_LOAD = 600;
constexpr int T_X1_H = 70;
constexpr int T_GU_X2 = 140;
constexpr int T_WO_X2 = 265;
constexpr int T_Q_O = 215;
constexpr int T_Q_X3 = 330;
constexpr int T_O_Q = 380;
constexpr int DLY = 11;
constexpr int NPROBE = 4;

__device__ __forceinline__ f4 mfma(v4u a, v4u b, f4 c) {
  return __builtin_amdgcn_mfma_f32_16x16x32_bf16(__builtin_bit_cast(bf8, a), __builtin_bit_cast(bf8, b), c, 0, 0, 0);
}
__device__ __forceinline__ unsigned hi16pack(float hi, float lo) {
  return __builtin_amdgcn_perm(__builtin_bit_cast(unsigned, hi), __builtin_bit_cast(unsigned, lo), 0x07060302u);
}
__device__ __forceinline__ unsigned pack_bf16(float lo, float hi) {
  bf2 r = {(__bf16)lo, (__bf16)hi};
  unsigned u = __builtin_bit_cast(unsigned, r);
  return u == SENT ? 0x7FFF7FFFu : u;
}
__device__ __forceinline__ float bflo(unsigned u) { return __builtin_bit_cast(float, u << 16); }
__device__ __forceinline__ float bfhi(unsigned u) { return __builtin_bit_cast(float, u & 0xFFFF0000u); }
__device__ __forceinline__ float dot2(unsigned a, unsigned b, float c) {
  return __builtin_amdgcn_fdot2_f32_bf16(__builtin_bit_cast(bf2, a), __builtin_bit_cast(bf2, b), c, false);
}
template <int CTRL> __device__ __forceinline__ float dpp(float x) {
  return __builtin_bit_cast(float, __builtin_amdgcn_mov_dpp(__builtin_bit_cast(int, x), CTRL, 0xf, 0xf, true));
}
constexpr int XOR1 = 0xB1, XOR2 = 0x4E, ROR4 = 0x124, ROR8 = 0x128;
__device__ __forceinline__ float xrow_sum(float x) {
  auto s = __builtin_amdgcn_permlane16_swap(__float_as_uint(x), __float_as_uint(x), false, false);
  x = __uint_as_float(s[0]) + __uint_as_float(s[1]);
  auto t = __builtin_amdgcn_permlane32_swap(__float_as_uint(x), __float_as_uint(x), false, false);
  return __uint_as_float(t[0]) + __uint_as_float(t[1]);
}
__device__ __forceinline__ float xrow_max(float x) {
  auto s = __builtin_amdgcn_permlane16_swap(__float_as_uint(x), __float_as_uint(x), false, false);
  x = fmaxf(__uint_as_float(s[0]), __uint_as_float(s[1]));
  auto t = __builtin_amdgcn_permlane32_swap(__float_as_uint(x), __float_as_uint(x), false, false);
  return fmaxf(__uint_as_float(t[0]), __uint_as_float(t[1]));
}
__device__ __forceinline__ float x32_sum(float x) {
  auto t = __builtin_amdgcn_permlane32_swap(__float_as_uint(x), __float_as_uint(x), false, false);
  return __uint_as_float(t[0]) + __uint_as_float(t[1]);
}
__device__ __forceinline__ rsrc_t mkrsrc(const void* p, unsigned bytes) {
  return __builtin_amdgcn_make_buffer_rsrc((void*)p, 0, bytes, 0x00020000);
}
template <int AUX> __device__ __forceinline__ v4u ld128(rsrc_t rs, int voff, int soff) {
  return __builtin_amdgcn_raw_buffer_load_b128(rs, voff, soff, AUX);
}
__device__ __forceinline__ unsigned umax3(unsigned a, unsigned b, unsigned c) { return max(max(a, b), c); }
__device__ __forceinline__ unsigned vmax4(v4u x, unsigned m) { return umax3(x.x, x.y, umax3(x.z, x.w, m)); }

__device__ __forceinline__ unsigned long long now() { return __builtin_amdgcn_s_memrealtime(); }
__device__ __forceinline__ void hold(unsigned long long t0, int ticks) {
  if (ticks > 0) while ((long long)(now() - t0) < ticks) __builtin_amdgcn_s_sleep(1);
}
#define SETTLE() __builtin_amdgcn_s_sleep(DLY)
__device__ __forceinline__ int xcc_id() { return __builtin_amdgcn_s_getreg(20 | (3 << 11)) & 7; }
__device__ __forceinline__ void near_probe(rsrc_t rws, int slot, unsigned tag, int l) {
  unsigned best0 = ~0u, best1 = ~0u;
#pragma unroll 1
  for (int i = 0; i < NPROBE; ++i) {
    unsigned long long t0, t1, t2;
    const int o0 = l == 0 ? dup(OFF_PROBE + slot * 128, 0) : OOB, o1 = l == 0 ? dup(OFF_PROBE + slot * 128, 1) : OOB;
    asm volatile("s_waitcnt vmcnt(0)\n\ts_memtime %0\n\ts_waitcnt lgkmcnt(0)\n\tbuffer_store_dword %3, %4, %6, 0 offen sc1\n\ts_waitcnt vmcnt(0)\n\t"
                 "s_memtime %1\n\ts_waitcnt lgkmcnt(0)\n\tbuffer_store_dword %3, %5, %6, 0 offen sc1\n\ts_waitcnt vmcnt(0)\n\ts_memtime %2\n\ts_waitcnt lgkmcnt(0)"
                 : "=&s"(t0), "=&s"(t1), "=&s"(t2) : "v"(i), "v"(o0), "v"(o1), "s"(rws) : "memory");
    best0 = min(best0, (unsigned)(t1 - t0)); best1 = min(best1, (unsigned)(t2 - t1));
  }
  if (l == 0) __builtin_amdgcn_raw_buffer_store_b32((tag << 1) | (best1 < best0 ? 1u : 0u), rws, OFF_NEAR + xcc_id() * 4, 0, AUX_SC1);
}
__device__ __forceinline__ int near_fetch(rsrc_t rws, unsigned tag) {
  const unsigned v = __builtin_amdgcn_readfirstlane(__builtin_amdgcn_raw_buffer_load_b32(rws, OFF_NEAR + xcc_id() * 4, 0, AUX_SC1));
  return (v >> 1) == (tag & 0x7FFFFFFFu) ? (int)(v & 1u) : 0;
}
template <int N> __device__ __forceinline__ void gather(v4u (&x)[N], rsrc_t rs, const int (&off_)[N], bool& dead, int nc) {
  int off[N];
#pragma unroll
  for (int i = 0; i < N; ++i) off[i] = dup(off_[i], nc);
  for (int spin = 0;; ++spin) {
    asm volatile("" ::: "memory");
#pragma unroll
    for (int i = 0; i < N; ++i) x[i] = ld128<AUX_SC1>(rs, off[i], 0);
    unsigned m = 0u;
#pragma unroll
    for (int i = 0; i < N; ++i) m = vmax4(x[i], m);
    if (!__any(m == SENT)) break;
    if (dead || spin > SPIN_CAP) { dead = true; break; }
  }
}
__device__ __forceinline__ void clear16(rsrc_t rws, int off) {
  __builtin_amdgcn_raw_buffer_store_b128(v4u{SENT, SENT, SENT, SENT}, rws, dup(off, 0), 0, AUX_SC1);
  __builtin_amdgcn_raw_buffer_store_b128(v4u{SENT, SENT, SENT, SENT}, rws, dup(off, 1), 0, AUX_SC1);
}
__device__ __forceinline__ void flags_wait(rsrc_t rws, unsigned long long tag, int l, bool& dead) {
  const unsigned lo = (unsigned)tag, hi = (unsigned)(tag >> 32);
  for (int spin = 0;; ++spin) {
    asm volatile("" ::: "memory");
    const v4u a = ld128<AUX_SC1>(rws, OFF_FLAG + l * 32, 0), b = ld128<AUX_SC1>(rws, OFF_FLAG + l * 32 + 16, 0);
    const bool ok = (a.x == lo) & (a.y == hi) & (a.z == lo) & (a.w == hi) & (b.x == lo) & (b.y == hi) & (b.z == lo) & (b.w == hi);
    if (__all(ok)) break;
    if (dead || spin > SPIN_CAP) { dead = true; break; }
    __builtin_amdgcn_s_sleep(4);
  }
}
__device__ __forceinline__ v4u xpose16(v4u raw, v4u sel1, v4u sel2) {
  const f4 z = {0.f, 0.f, 0.f, 0.f};
  const f4 d1 = mfma(raw, sel1, z), d2 = mfma(raw, sel2, z);
  return v4u{hi16pack(d1[1], d1[0]), hi16pack(d1[3], d1[2]), hi16pack(d2[1], d2[0]), hi16pack(d2[3], d2[2])};
}
__device__ __forceinline__ void xpose32(v4u& a, v4u& b, v4u sel1, v4u sel2) {
  const f4 z = {0.f, 0.f, 0.f, 0.f};
  const f4 a1 = mfma(a, sel1, z), b1 = mfma(b, sel1, z), a2 = mfma(a, sel2, z), b2 = mfma(b, sel2, z);
  a = v4u{hi16pack(a1[1], a1[0]), hi16pack(a1[3], a1[2]), hi16pack(b1[1], b1[0]), hi16pack(b1[3], b1[2])};
  b = v4u{hi16pack(a2[1], a2[0]), hi16pack(a2[3], a2[2]), hi16pack(b2[1], b2[0]), hi16pack(b2[3], b2[2])};
}

#define ASM_DMA(voff, rs, soff, ldsa) \
  asm volatile("s_mov_b32 m0, %3\n\ts_nop 0\n\tbuffer_load_dwordx4 %0, %1, %2 offen nt lds" ::"v"(voff), "s"(rs), "s"(soff), "s"(ldsa) : "memory")
#define WAIT_VM(n) asm volatile("s_waitcnt vmcnt(" #n ")" ::: "memory")
#define PIN8(a, o) asm volatile("" : "+v"(a[o]), "+v"(a[o + 1]), "+v"(a[o + 2]), "+v"(a[o + 3]), "+v"(a[o + 4]), "+v"(a[o + 5]), "+v"(a[o + 6]), "+v"(a[o + 7]))

__device__ __forceinline__ v2u tr_read(LAS unsigned char* p) {
  s4 t = __builtin_amdgcn_ds_read_tr16_b64_v4i16((LAS s4*)p);
  return __builtin_bit_cast(v2u, t);
}

__device__ __forceinline__ void attn_math(const int PASS, LAS unsigned char* L, const v4u (&kf)[32], const v4u (&vf)[16], const v4u (&qf)[4],
                                          rsrc_t rws, int b, int h, int w, int l_) {
  int l = l_; asm volatile("" : "+v"(l));
  const int g = l >> 4;
  const f4 z = {0.f, 0.f, 0.f, 0.f};
  constexpr float CS = 0.08838834764831845f * 1.4426950408889634f;
  float s0 = 0.f, s1 = 0.f;
#pragma unroll
  for (int t = 0; t < 8; t += 2) {
    f4 d0 = z, d1 = z;
#pragma unroll
    for (int s = 0; s < 4; ++s) { d0 = mfma(qf[s], kf[t * 4 + s], d0); d1 = mfma(qf[s], kf[t * 4 + 4 + s], d1); }
    if (t < 4) { s0 = (g == t) ? d0[0] : s0; s0 = (g == t + 1) ? d1[0] : s0; }
    else { s1 = (g == t - 4) ? d0[0] : s1; s1 = (g == t - 3) ? d1[0] : s1; }
  }
  float m = fmaxf(s0, s1);
  m = fmaxf(m, dpp<XOR1>(m)); m = fmaxf(m, dpp<XOR2>(m)); m = fmaxf(m, dpp<ROR4>(m)); m = fmaxf(m, dpp<ROR8>(m));
  m = xrow_max(m);
  const __bf16 p0 = (__bf16)__builtin_amdgcn_exp2f((s0 - m) * CS), p1 = (__bf16)__builtin_amdgcn_exp2f((s1 - m) * CS);
  LAS __bf16* pw = (LAS __bf16*)(L + L_P + w * 256);
  pw[l] = p0; pw[64 + l] = p1;
  v4u pf[4];
#pragma unroll
  for (int s = 0; s < 4; ++s) pf[s] = *(LAS v4u*)(L + L_P + w * 256 + s * 64 + g * 16);
  float ls = (float)p0 + (float)p1;
  ls += dpp<XOR1>(ls); ls += dpp<XOR2>(ls); ls += dpp<ROR4>(ls); ls += dpp<ROR8>(ls);
  ls = xrow_sum(ls);
  float oa0 = 0.f, oa1 = 0.f;
#pragma unroll
  for (int n = 0; n < 8; n += 2) {
    f4 d0 = mfma(pf[0], vf[n], z), d1 = mfma(pf[0], vf[n + 1], z);
    d0 = mfma(pf[1], vf[8 + n], d0); d1 = mfma(pf[1], vf[9 + n], d1);
    if (n < 4) { oa0 = (g == n) ? d0[0] : oa0; oa0 = (g == n + 1) ? d1[0] : oa0; }
    else { oa1 = (g == n - 4) ? d0[0] : oa1; oa1 = (g == n - 3) ? d1[0] : oa1; }
  }
  const int q4 = (l >> 2) & 3;
  const int trb = L_V + w * 16384 + (8 * g + q4) * 256 + ((q4 | ((g & 1) << 2)) << 5) + (l & 3) * 8;
  float ob0 = 0.f, ob1 = 0.f;
  if (PASS == 0) {
#pragma unroll
    for (int hb = 0; hb < 2; ++hb) {
      if (hb == 0) WAIT_VM(8); else WAIT_VM(0);
      v2u t0[4], t1[4];
      float pc0 = 0.f, pc1 = 0.f;
#define TR_LOAD(n) do { LAS unsigned char* a_ = L + (trb ^ ((n) << 5)) + hb * 8192; t0[(n) & 3] = tr_read(a_); t1[(n) & 3] = tr_read(a_ + 1024); } while (0)
      TR_LOAD(0); TR_LOAD(1);
#pragma unroll
      for (int n = 0; n < 8; n += 2) {
        if (n < 6) { TR_LOAD(n + 2); TR_LOAD(n + 3); }
        __builtin_amdgcn_sched_barrier(0);
        const f4 d0 = mfma(pf[2 + hb], v4u{t0[n & 3].x, t0[n & 3].y, t1[n & 3].x, t1[n & 3].y}, z);
        const f4 d1 = mfma(pf[2 + hb], v4u{t0[(n + 1) & 3].x, t0[(n + 1) & 3].y, t1[(n + 1) & 3].x, t1[(n + 1) & 3].y}, z);
        if (n < 4) { pc0 = (g == n) ? d0[0] : pc0; pc0 = (g == n + 1) ? d1[0] : pc0; }
        else { pc1 = (g == n - 4) ? d0[0] : pc1; pc1 = (g == n - 3) ? d1[0] : pc1; }
      }
      ob0 += pc0; ob1 += pc1;
    }
  } else {
    v2u t0[2], t1[2], t2[2], t3[2];
#define TR_LOAD2(n) do { LAS unsigned char* a_ = L + (trb ^ ((n) << 5)); \
      t0[(n) & 1] = tr_read(a_); t1[(n) & 1] = tr_read(a_ + 1024); t2[(n) & 1] = tr_read(a_ + 8192); t3[(n) & 1] = tr_read(a_ + 8192 + 1024); } while (0)
    TR_LOAD2(0);
#pragma unroll
    for (int n = 0; n < 8; ++n) {
      if (n < 7) TR_LOAD2(n + 1);
      __builtin_amdgcn_sched_barrier(0);
      f4 d = mfma(pf[2], v4u{t0[n & 1].x, t0[n & 1].y, t1[n & 1].x, t1[n & 1].y}, z);
      d = mfma(pf[3], v4u{t2[n & 1].x, t2[n & 1].y, t3[n & 1].x, t3[n & 1].y}, d);
      if (n < 4) ob0 = (g == n) ? d[0] : ob0; else ob1 = (g == n - 4) ? d[0] : ob1;
    }
  }
  LAS float* ow = (LAS float*)(L + L_OW);
  ow[w * 128 + l] = oa0 + ob0; ow[w * 128 + 64 + l] = oa1 + ob1;
  if (l == 0) *(LAS f2*)(L + L_ML + w * 8) = f2{m, ls};
  __syncthreads();
  if (w == 0) {
    f4 ml[4];
    f2 ov[8];
#pragma unroll
    for (int i = 0; i < 4; ++i) ml[i] = *(LAS f4*)(L + L_ML + i * 16);
#pragma unroll
    for (int i = 0; i < 8; ++i) ov[i] = *(LAS f2*)(L + L_OW + i * 512 + l * 8);
    float M = fmaxf(fmaxf(fmaxf(ml[0][0], ml[0][2]), fmaxf(ml[1][0], ml[1][2])), fmaxf(fmaxf(ml[2][0], ml[2][2]), fmaxf(ml[3][0], ml[3][2])));
    float Ls = 0.f, o0 = 0.f, o1 = 0.f;
#pragma unroll
    for (int i = 0; i < 8; ++i) {
      const float f = __builtin_amdgcn_exp2f((ml[i >> 1][(i & 1) * 2] - M) * CS);
      Ls = fmaf(f, ml[i >> 1][(i & 1) * 2 + 1], Ls); o0 = fmaf(f, ov[i].x, o0); o1 = fmaf(f, ov[i].y, o1);
    }
    const float inv = __builtin_amdgcn_rcpf(Ls);
    const unsigned ob = pack_bf16(o0 * inv, o1 * inv);
    const int oo = OFF_O + PASS * SZ_O + (h * 4 + b) * 256 + l * 4;
    __builtin_amdgcn_raw_buffer_store_b32(ob, rws, dup(oo, 0), 0, AUX_SC1);
    __builtin_amdgcn_raw_buffer_store_b32(ob, rws, dup(oo, 1), 0, AUX_SC1);
  }
}

__device__ __forceinline__ void attn_role(LAS unsigned char* L, const u16* K, const u16* V, rsrc_t rws, int cu, int xc, int y, int w, int l) {
  const int h = 4 * xc + (y >> 2), b = y & 3, g = l >> 4, r = l & 15;
  const size_t kv = ((size_t)(b * NH + h) * SK + (SK - WIN) + 128 * w) * DH;
  const rsrc_t rK = mkrsrc(K + kv, 32768), rV = mkrsrc(V + kv, 32768);
  bool dead = false;
  v4u kf[32], vf[16], qf[4];
  const int ldsV = L_V + w * 16384;
  const int ks0 = g * 256 + ((r ^ g) << 4);
#define KX(i) (((i) & 3) << 6)
  const int vs0 = g * 256 + (((r >> 1) ^ g) << 5) + (l & 1) * 16, vs1 = g * 256 + (((r >> 1) ^ (g | 4)) << 5) + (l & 1) * 16;
#define K_PIECE(n) _Pragma("unroll") for (int i = 0; i < 8; ++i) \
    ASM_DMA(ks0 ^ KX(i), rK, (n) * 8192 + i * 1024, ldsV + ((n) & 1) * 8192 + i * 1024)
#define V_PIECE(n, buf) _Pragma("unroll") for (int i = 0; i < 8; ++i) \
    ASM_DMA((i & 2) ? vs1 : vs0, rV, (n) * 8192 + i * 1024, ldsV + (buf) * 8192 + i * 1024)
#define K_READ(n) do { _Pragma("unroll") for (int t = 0; t < 2; ++t) _Pragma("unroll") for (int s = 0; s < 4; ++s) \
      kf[((n) * 2 + t) * 4 + s] = *(LAS v4u*)(L + ldsV + ((n) & 1) * 8192 + (16 * t + r) * 256 + (((4 * s + g) ^ r) << 4)); \
    asm volatile("s_waitcnt lgkmcnt(0)" ::: "memory"); PIN8(kf, (n) * 8); } while (0)
  const int trb = ldsV + (8 * g + ((l >> 2) & 3)) * 256 + ((((l >> 2) & 3) | ((g & 1) << 2)) << 5) + (l & 3) * 8;
#define V_READ(n) do { _Pragma("unroll") for (int c8 = 0; c8 < 8; ++c8) { \
      LAS unsigned char* a = L + (trb ^ (c8 << 5)) + (n) * 8192; \
      const v2u t0 = tr_read(a), t1 = tr_read(a + 1024); \
      vf[(n) * 8 + c8] = v4u{t0.x, t0.y, t1.x, t1.y}; } \
    asm volatile("s_waitcnt lgkmcnt(0)" ::: "memory"); PIN8(vf, (n) * 8); } while (0)
  K_PIECE(0);
  if (threadIdx.x == 0) *(volatile LAS unsigned*)(L + L_QF) = 0u;
  asm volatile("s_waitcnt lgkmcnt(0)\n\ts_barrier" ::: "memory");
  v2u ep = {0u, 0u};
  unsigned long long tag = 0;
  {
    const v4u sent = {SENT, SENT, SENT, SENT};
    const int co = dup(OFF_O + ((l >> 4) & 1) * SZ_O + (h * 4 + b) * 256 + r * 16, l >> 5);
    asm volatile("s_cmp_eq_u32 %5, 0\n\ts_cbranch_scc0 1f\n\tbuffer_load_dwordx2 %0, %1, %4, 0 offen sc1\n\t"
                 "buffer_store_dwordx4 %2, %3, %4, 0 offen sc1\n\ts_nop 1\n1:"
                 : "+v"(ep) : "v"(OFF_EP), "v"(sent), "v"(co), "s"(rws), "s"(w) : "memory", "scc");
  }
  K_PIECE(1);
  WAIT_VM(8);
  asm volatile("" : "+v"(ep));
  if (w == 0) {
    tag = ((unsigned long long)(unsigned)__builtin_amdgcn_readfirstlane(ep.y) << 32 | (unsigned)__builtin_amdgcn_readfirstlane(ep.x)) + 1ull;
    const v2u tg = {(unsigned)tag, (unsigned)(tag >> 32)};
    asm volatile("buffer_store_dwordx2 %0, %1, %2, 0 offen sc1\n\ts_nop 0" ::"v"(tg), "v"(l == 0 ? OFF_FLAG + cu * 8 : OOB), "s"(rws) : "memory");
  }
  K_READ(0); K_PIECE(2);
  WAIT_VM(8); K_READ(1); K_PIECE(3);
  WAIT_VM(8); K_READ(2); V_PIECE(0, 0);
  WAIT_VM(8); K_READ(3); V_PIECE(1, 1);
  if (w == 0) {
    flags_wait(rws, tag, l, dead);
    v4u q1[1];
    const int off[1] = {l < 16 ? OFF_Q + (h * 16 + b) * 64 + (l >> 2) * 256 + (l & 3) * 16 : OOB};
    gather<1>(q1, rws, off, dead, 0);
    if (l < 16) *(LAS v4u*)(L + L_Q + l * 16) = q1[0];
    asm volatile("s_waitcnt lgkmcnt(0)" ::: "memory");
    if (l == 0) *(volatile LAS unsigned*)(L + L_QF) = 1u;
  }
  WAIT_VM(8); V_READ(0); V_PIECE(2, 0);
  WAIT_VM(8); V_READ(1); V_PIECE(3, 1);
#pragma unroll 1
  for (int pass = 0; pass < 2; ++pass) {
    if (pass == 0) {
      for (int spin = 0; *(volatile LAS unsigned*)(L + L_QF) == 0u; ++spin) {
        if (spin > (SPIN_CAP << 4)) { dead = true; break; }
        __builtin_amdgcn_s_sleep(1);
      }
#pragma unroll
      for (int s = 0; s < 4; ++s) qf[s] = *(LAS v4u*)(L + L_Q + s * 64 + g * 16);
      asm volatile("" ::: "memory");
    } else {
      const unsigned long long tr = now();
      if (w == 0) { const int n_ = near_fetch(rws, (unsigned)tag); if (l == 0) *(volatile LAS unsigned*)(L + L_QF + 4) = n_; }
      __syncthreads();
      const int nc = __builtin_amdgcn_readfirstlane(*(volatile LAS unsigned*)(L + L_QF + 4));
      hold(tr, T_O_Q);
      int lo = l; asm volatile("" : "+v"(lo));
      const int qo = OFF_Q + SZ_Q + (h * 16 + b) * 64 + (lo >> 4) * 16;
      const int off[4] = {qo, qo + 256, qo + 512, qo + 768};
      gather<4>(qf, rws, off, dead, nc);
    }
    attn_math(pass, L, kf, vf, qf, rws, b, h, w, l);
  }
}

__device__ __forceinline__ f4 red_sum(LAS unsigned char* L, int row, int l) {
  const int hf = l >> 5, cg = l & 7;
  f4 s = *(LAS f4*)(L + L_RED + (hf * 4) * RED_W + row * 144 + cg * 16);
#pragma unroll
  for (int i = 1; i < 4; ++i) s += *(LAS f4*)(L + L_RED + (hf * 4 + i) * RED_W + row * 144 + cg * 16);
  return f4{x32_sum(s[0]), x32_sum(s[1]), x32_sum(s[2]), x32_sum(s[3])};
}
__device__ __forceinline__ f2 red_stat(LAS unsigned char* L, int row, int l) {
  const int hf = l >> 5;
  f2 s = *(LAS f2*)(L + L_RED + (hf * 4) * RED_W + row * 144 + 128);
#pragma unroll
  for (int i = 1; i < 4; ++i) s += *(LAS f2*)(L + L_RED + (hf * 4 + i) * RED_W + row * 144 + 128);
  return f2{x32_sum(s[0]), x32_sum(s[1])};
}

__device__ __forceinline__ f4 red_sum_gu(LAS unsigned char* L, int row, int l) {
  const int m = l >> 5, cg = l & 7;
  f4 s = *(LAS f4*)(L + L_RED + m * RED_W + row * 144 + cg * 16);
#pragma unroll
  for (int i = 1; i < 4; ++i) s += *(LAS f4*)(L + L_RED + (2 * i + m) * RED_W + row * 144 + cg * 16);
  return s;
}
struct WArgs { const u16 *x, *wq, *wo, *wg, *wu, *wd; u16* out; };

template <int PASS>
__device__ __forceinline__ void q_phase(LAS unsigned char* L, const v4u (&xf)[3], const v4u (&wqf)[6], f4 csq, rsrc_t rws, int tq, int w, int l) {
  const int g = l >> 4, c = l & 15;
  const f4 z = {0.f, 0.f, 0.f, 0.f};
  const v4u ones = {0x3F803F80u, 0x3F803F80u, 0x3F803F80u, 0x3F803F80u};
  f4 d0 = z, d1 = z, ds = z;
  float ss = 0.f;
#pragma unroll
  for (int i = 0; i < 3; ++i) {
    d0 = mfma(wqf[2 * i], xf[i], d0); d1 = mfma(wqf[2 * i + 1], xf[i], d1); ds = mfma(ones, xf[i], ds);
    ss = dot2(xf[i].x, xf[i].x, ss); ss = dot2(xf[i].y, xf[i].y, ss); ss = dot2(xf[i].z, xf[i].z, ss); ss = dot2(xf[i].w, xf[i].w, ss);
  }
  ss = xrow_sum(ss);
  LAS unsigned char* R = L + L_RED + w * RED_W + c * 144;
  if (c < 4) {
    *(LAS f4*)(R + g * 16) = d0; *(LAS f4*)(R + 64 + g * 16) = d1;
    if (g == 0) *(LAS f2*)(R + 128) = f2{ds[0], ss};
  }
  __syncthreads();
  if (w == 0) {
    const int b = (l >> 3) & 3, cg = l & 7;
    const f4 p = red_sum(L, b, l);
    const f2 s = red_stat(L, b, l);
    const float mu = s[0] * (1.0f / D), var = fmaxf(s[1] * (1.0f / D) - mu * mu, 0.f), rs = __builtin_amdgcn_rsqf(var + 1e-5f);
    {
      const v2u o = {pack_bf16((p[0] - mu * csq[0]) * rs, (p[1] - mu * csq[1]) * rs), pack_bf16((p[2] - mu * csq[2]) * rs, (p[3] - mu * csq[3]) * rs)};
      __builtin_amdgcn_raw_buffer_store_b64(o, rws, dup(OFF_Q + PASS * SZ_Q + tq * 256 + b * 64 + cg * 8, l >> 5), 0, AUX_SC1);
    }
  }
  __syncthreads();
}

__device__ __forceinline__ void q_second(LAS unsigned char* L, const v4u (&wqf)[6], f4 csq, rsrc_t rws, int tq, int w, int l, bool& dead, int nc) {
  const int g = l >> 4, c = l & 15;
  v4u xf[3];
  int off[3];
#pragma unroll
  for (int i = 0; i < 3; ++i) {
    const int s = w + 8 * i;
    off[i] = (c < 4 && s < 20) ? OFF_X2 + ((2 * s + (g >> 1)) * 4 + c) * 32 + (g & 1) * 16 : OOB;
  }
  gather<3>(xf, rws, off, dead, nc);
  q_phase<1>(L, xf, wqf, csq, rws, tq, w, l);
}

__device__ __forceinline__ void weight_role(LAS unsigned char* L, const WArgs& a, rsrc_t rws, int cu, int xc, int u, int w, int l) {
  const int g = l >> 4, r = l & 15, c = r;
  const f4 z = {0.f, 0.f, 0.f, 0.f};
  const v4u ones = {0x3F803F80u, 0x3F803F80u, 0x3F803F80u, 0x3F803F80u};
  v4u sel1, sel2;
  {
    const int j = r - 8 * (g & 1);
    const unsigned one = (j & 1) ? 0x3F800000u : 0x00003F80u;
    const bool in = (unsigned)j < 8u;
    v4u s = {(in && (j >> 1) == 0) ? one : 0u, (in && (j >> 1) == 1) ? one : 0u, (in && (j >> 1) == 2) ? one : 0u, (in && (j >> 1) == 3) ? one : 0u};
    const v4u zz = {0u, 0u, 0u, 0u};
    sel1 = g < 2 ? s : zz; sel2 = g < 2 ? zz : s;
  }
  const int tq = xc * 16 + u;
  const bool has_wo = u < 10, has_gu = u < (xc < 3 ? 8 : 6), has_dn = u >= 10 && u < 15;
  const int wo_id = xc * 10 + u, kq = wo_id / 20, nt32 = wo_id % 20;
  const int t32 = xc < 3 ? xc * 8 + u : 24 + (xc - 3) * 6 + u;
  const int td = xc * 5 + (u - 10);
  const int rho16 = 8 * (r >> 2) + (r & 3);
  const int rho32 = rho16 + 4 * (g >> 1);

  bool dead = false;
  const unsigned long long tstart = now();
  v2u ep = {0u, 0u};
  if (w == 0) {
    ep = __builtin_amdgcn_raw_buffer_load_b64(rws, OFF_EP, 0, AUX_SC1);
    const int hf = l >> 5, k = l & 31;
    clear16(rws, k < 16 ? OFF_Q + hf * SZ_Q + tq * 256 + k * 16 : has_wo ? OFF_X1 + hf * SZ_X1 + (kq * 40 + 2 * nt32) * 128 + (k - 16) * 16 : OOB);
    clear16(rws, (k < 16 && has_gu) ? OFF_H + hf * SZ_H + t32 * 256 + k * 16 : (k < 8 && hf == 0 && has_dn) ? OFF_X2 + td * 128 + k * 16 : OOB);
  }
  v4u wqf[6];
  unsigned long long tag = 0;
  f4 csq;
  {
    v4u xf[3];
    const rsrc_t rx = mkrsrc(a.x, NB * D * 2), rq = mkrsrc(a.wq, D * HD * 2);
#pragma unroll
    for (int i = 0; i < 3; ++i) {
      const int s = w + 8 * i;
      xf[i] = ld128<0>(rx, (c < 4 && s < 20) ? c * 1280 + s * 64 + g * 16 : OOB, 0);
    }
#pragma unroll
    for (int i = 0; i < 3; ++i) {
      const int s = w + 8 * i;
      const int vo = s < 20 ? (32 * s + rho16) * 8192 + tq * 64 + g * 16 : OOB;
      wqf[2 * i] = ld128<AUX_NT>(rq, vo, 0); wqf[2 * i + 1] = ld128<AUX_NT>(rq, vo, 4 * 8192);
    }
    __builtin_amdgcn_sched_barrier(0);
    if (w == 0) {
      asm volatile("s_waitcnt vmcnt(9)" : "+v"(ep)::"memory");
      tag = ((unsigned long long)(unsigned)__builtin_amdgcn_readfirstlane(ep.y) << 32 | (unsigned)__builtin_amdgcn_readfirstlane(ep.x)) + 1ull;
      if (l == 0) __builtin_amdgcn_raw_buffer_store_b64(v2u{(unsigned)tag, (unsigned)(tag >> 32)}, rws, OFF_FLAG + cu * 8, 0, AUX_SC1);
    }
#pragma unroll
    for (int i = 0; i < 3; ++i) xpose32(wqf[2 * i], wqf[2 * i + 1], sel1, sel2);
    f4 c0 = z, c1 = z;
#pragma unroll
    for (int i = 0; i < 3; ++i) { c0 = mfma(wqf[2 * i], ones, c0); c1 = mfma(wqf[2 * i + 1], ones, c1); }
    LAS unsigned char* R4 = L + L_RED + w * RED_W + 4 * 144;
    if (c == 0) { *(LAS f4*)(R4 + g * 16) = c0; *(LAS f4*)(R4 + 64 + g * 16) = c1; }
    __syncthreads();
    csq = red_sum(L, 4, l);
    q_phase<0>(L, xf, wqf, csq, rws, tq, w, l);
  }
  const int fb = (l >> 3) & 3, fcg = l & 7;
  LAS unsigned char* R = L + L_RED + w * RED_W + c * 144;

  if (has_wo) {
    v4u wof[8], guf[10];
    const int gm = (w ^ (w >> 2)) & 1, gq = w >> 1;
    LAS unsigned char* Rg = L + L_RED + (2 * gq + gm) * RED_W + c * 144;
    {
      const rsrc_t ro = mkrsrc(a.wo, D * HD * 2), rgu = mkrsrc(gm ? a.wu : a.wg, D * FF * 2);
      const int vo = ((8 * kq + w) * 128 + rho16) * 1280 + nt32 * 64 + g * 16;
#pragma unroll
      for (int s = 0; s < 4; ++s) { wof[2 * s] = ld128<AUX_NT>(ro, vo, s * 32 * 1280); wof[2 * s + 1] = ld128<AUX_NT>(ro, vo, (s * 32 + 4) * 1280); }
      const int vg = has_gu ? (160 * gq + rho16) * 3456 + t32 * 64 + g * 16 : OOB;
#pragma unroll
      for (int i = 0; i < 5; ++i) { guf[2 * i] = ld128<AUX_NT>(rgu, vg, i * 32 * 3456); guf[2 * i + 1] = ld128<AUX_NT>(rgu, vg, (i * 32 + 4) * 3456); }
      __builtin_amdgcn_sched_barrier(0);
    }
#pragma unroll
    for (int s = 0; s < 4; ++s) xpose32(wof[2 * s], wof[2 * s + 1], sel1, sel2);
#pragma unroll
    for (int i = 0; i < 5; ++i) xpose32(guf[2 * i], guf[2 * i + 1], sel1, sel2);
    f4 csg;
    {
      f4 c0 = z, c1 = z;
#pragma unroll
      for (int i = 0; i < 5; ++i) { c0 = mfma(guf[2 * i], ones, c0); c1 = mfma(guf[2 * i + 1], ones, c1); }
      LAS unsigned char* R4 = L + L_RED + (2 * gq + gm) * RED_W + 4 * 144;
      if (c == 0) { *(LAS f4*)(R4 + g * 16) = c0; *(LAS f4*)(R4 + 64 + g * 16) = c1; }
      if (w == 0) flags_wait(rws, tag, l, dead);
      __syncthreads();
      csg = red_sum_gu(L, 4, l);
    }
    hold(tstart, T_NEAR);
    if (w == 0) { const int n_ = near_fetch(rws, (unsigned)tag); if (l == 0) *(volatile LAS unsigned*)(L + L_NEAR) = n_; }
    __syncthreads();
    const int nc = __builtin_amdgcn_readfirstlane(*(volatile LAS unsigned*)(L + L_NEAR));
    hold(tstart, HOLD_T);
#pragma unroll 1
    for (int pass = 0; pass < 2; ++pass) {
      {
        v4u of[4];
        int lo = l; asm volatile("" : "+v"(lo));
        const int o0 = (lo & 15) < 4 ? OFF_O + pass * SZ_O + ((8 * kq + w) * 4 + (lo & 15)) * 256 + (lo >> 4) * 16 : OOB;
        const int off[4] = {o0, o0 + 64, o0 + 128, o0 + 192};
        v2u res = {0u, 0u};
        if (w == 0 && kq == 0) {
          if (pass == 0) res = *(const v2u*)(a.x + fb * D + nt32 * 32 + fcg * 4);
          else res = __builtin_amdgcn_raw_buffer_load_b64(rws, dup(OFF_X2 + ((2 * nt32 + (fcg >> 2)) * 4 + fb) * 32 + (fcg & 3) * 8, nc), 0, AUX_SC1);
        }
        gather<4>(of, rws, off, dead, nc);
        f4 d0 = z, d1 = z;
#pragma unroll
        for (int s = 0; s < 4; ++s) { d0 = mfma(wof[2 * s], of[s], d0); d1 = mfma(wof[2 * s + 1], of[s], d1); }
        if (c < 4) { *(LAS f4*)(R + g * 16) = d0; *(LAS f4*)(R + 64 + g * 16) = d1; }
        __syncthreads();
        if (w == 0) {
          f4 p = red_sum(L, fb, l);
          p[0] += bflo(res.x); p[1] += bfhi(res.x); p[2] += bflo(res.y); p[3] += bfhi(res.y);
          {
            const v2u o = {pack_bf16(p[0], p[1]), pack_bf16(p[2], p[3])};
            __builtin_amdgcn_raw_buffer_store_b64(o, rws, dup(OFF_X1 + pass * SZ_X1 + ((kq * 40 + 2 * nt32 + (fcg >> 2)) * 4 + fb) * 32 + (fcg & 3) * 8, l >> 5), 0, AUX_SC1);
          }
        }
        __syncthreads();
      }
      if (has_gu) {
        v4u x1[5];
        int xo[5];
        int lo = l; asm volatile("" : "+v"(lo));
#pragma unroll
        for (int i = 0; i < 5; ++i)
          xo[i] = OFF_X1 + pass * SZ_X1 + ((((lo & 15) >> 2) * 40 + 2 * (5 * gq + i) + (lo >> 5)) * 4 + (lo & 3)) * 32 + ((lo >> 4) & 1) * 16;
        SETTLE();
        gather<5>(x1, rws, xo, dead, nc);
        f4 d0 = z, d1 = z, ds = z;
#pragma unroll
        for (int i = 0; i < 5; ++i) { d0 = mfma(guf[2 * i], x1[i], d0); d1 = mfma(guf[2 * i + 1], x1[i], d1); }
        if (gm == 0) {
#pragma unroll
          for (int i = 0; i < 5; ++i) ds = mfma(ones, x1[i], ds);
        }
#pragma unroll
        for (int i = 0; i < 4; ++i) {
          d0[i] += dpp<ROR4>(d0[i]); d0[i] += dpp<ROR8>(d0[i]);
          d1[i] += dpp<ROR4>(d1[i]); d1[i] += dpp<ROR8>(d1[i]);
        }
        float sx = ds[0]; sx += dpp<ROR4>(sx); sx += dpp<ROR8>(sx);
        if (c < 4) {
          *(LAS f4*)(Rg + g * 16) = d0; *(LAS f4*)(Rg + 64 + g * 16) = d1;
          if (g == 0) *(LAS float*)(Rg + 128) = sx;
        }
        __syncthreads();
        if (w == 0) {
          const f4 p = red_sum_gu(L, fb, l);
          float sm = 0.f;
#pragma unroll
          for (int i = 0; i < 4; ++i) sm += *(LAS float*)(L + L_RED + 2 * i * RED_W + fb * 144 + 128);
          const float mu = sm * (1.0f / D);
          float hv[4];
#pragma unroll
          for (int i = 0; i < 4; ++i) {
            const float v = p[i] - mu * csg[i];
            auto sw = __builtin_amdgcn_permlane32_swap(__float_as_uint(v), __float_as_uint(v), false, false);
            hv[i] = fmaxf(__uint_as_float(sw[0]), 0.f) * __uint_as_float(sw[1]);
          }
          {
            const v2u o = {pack_bf16(hv[0], hv[1]), pack_bf16(hv[2], hv[3])};
            __builtin_amdgcn_raw_buffer_store_b64(o, rws, dup(OFF_H + pass * SZ_H + ((2 * t32 + (fcg >> 2)) * 4 + fb) * 32 + (fcg & 3) * 8, l >> 5), 0, AUX_SC1);
          }
        }
        __syncthreads();
      }
      if (pass == 0) { hold(now(), has_gu ? T_GU_X2 : T_WO_X2); q_second(L, wqf, csq, rws, tq, w, l, dead, nc); hold(now(), T_Q_O); }
    }
  } else {
    v4u wdf[7];
    if (!has_dn && w == 0) { near_probe(rws, xc, (unsigned)tag, l); }
    hold(tstart, T_DN_LOAD);
    {
      const rsrc_t rd = mkrsrc(a.wd, D * FF * 2);
#pragma unroll
      for (int i = 0; i < 7; ++i) {
        const int s = w + 8 * i;
        wdf[i] = ld128<AUX_NT>(rd, (has_dn && s < 54) ? (32 * s + rho32) * 1280 + td * 32 + (g & 1) * 16 : OOB, 0);
      }
      __builtin_amdgcn_sched_barrier(0);
    }
#pragma unroll
    for (int i = 0; i < 7; ++i) wdf[i] = xpose16(wdf[i], sel1, sel2);
    if (w == 0) {
      flags_wait(rws, tag, l, dead);
      if (has_dn && td == 0 && l == 0) __builtin_amdgcn_raw_buffer_store_b64(v2u{(unsigned)tag, (unsigned)(tag >> 32)}, rws, OFF_EP, 0, AUX_SC1);
    }
    __syncthreads();
    hold(tstart, T_NEAR);
    if (w == 0) { const int n_ = near_fetch(rws, (unsigned)tag); if (l == 0) *(volatile LAS unsigned*)(L + L_NEAR) = n_; }
    __syncthreads();
    const int nc = __builtin_amdgcn_readfirstlane(*(volatile LAS unsigned*)(L + L_NEAR));
    hold(tstart, HOLD_T);
#pragma unroll 1
    for (int pass = 0; pass < 2; ++pass) {
      if (has_dn) {
        v4u x1[3];
        int xoff[3];
        int lo = l; asm volatile("" : "+v"(lo));
#pragma unroll
        for (int i = 0; i < 3; ++i) {
          const int s = w + 8 * i;
          xoff[i] = s < 20 ? OFF_X1 + pass * SZ_X1 + ((((lo & 15) >> 2) * 40 + 2 * s + (lo >> 5)) * 4 + (lo & 3)) * 32 + ((lo >> 4) & 1) * 16 : OOB;
        }
        gather<3>(x1, rws, xoff, dead, nc);
        const unsigned long long tx1 = now();
        f4 gram = z, ds = z;
#pragma unroll
        for (int i = 0; i < 3; ++i) { gram = mfma(x1[i], x1[i], gram); ds = mfma(ones, x1[i], ds); }
        float sq = (c & 3) == 0 ? gram[0] : (c & 3) == 1 ? gram[1] : (c & 3) == 2 ? gram[2] : gram[3];
        sq += dpp<ROR4>(sq); sq += dpp<ROR8>(sq); sq = xrow_sum(sq);
        float sx = ds[0]; sx += dpp<ROR4>(sx); sx += dpp<ROR8>(sx);
        if (c < 4 && g == 0) *(LAS f2*)(R + 128) = f2{sx, sq};
        __syncthreads();
        float rs2 = 0.f; f4 res = z;
        if (w == 0) {
          v2u rp[4];
#pragma unroll
          for (int k = 0; k < 4; ++k)
            rp[k] = __builtin_amdgcn_raw_buffer_load_b64(rws, dup(OFF_X1 + pass * SZ_X1 + ((k * 40 + td) * 4 + fb) * 32 + (fcg & 3) * 8, nc), 0, AUX_SC1);
          const f2 s = red_stat(L, fb, l);
          const float mu = s[0] * (1.0f / D), var = fmaxf(s[1] * (1.0f / D) - mu * mu, 0.f);
          rs2 = __builtin_amdgcn_rcpf(var + 1e-5f);
#pragma unroll
          for (int k = 0; k < 4; ++k) { res[0] += bflo(rp[k].x); res[1] += bfhi(rp[k].x); res[2] += bflo(rp[k].y); res[3] += bfhi(rp[k].y); }
        }
        v4u hf[7];
        int hoff[7];
#pragma unroll
        for (int i = 0; i < 7; ++i) {
          const int s = w + 8 * i;
          hoff[i] = ((lo & 15) < 4 && s < 54) ? OFF_H + pass * SZ_H + ((2 * s + (lo >> 5)) * 4 + (lo & 15)) * 32 + ((lo >> 4) & 1) * 16 : OOB;
        }
        hold(tx1, T_X1_H);
        gather<7>(hf, rws, hoff, dead, nc);
        f4 d = z;
#pragma unroll
        for (int i = 0; i < 7; ++i) d = mfma(wdf[i], hf[i], d);
        if (c < 4) { *(LAS f4*)(R + g * 16) = d; *(LAS f4*)(R + 64 + g * 16) = z; }
        __syncthreads();
        if (w == 0) {
          const f4 p = red_sum(L, fb, l);
          if (fcg < 4) {
            f4 o;
#pragma unroll
            for (int i = 0; i < 4; ++i) o[i] = res[i] + rs2 * p[i];
            if (dead) o = f4{__builtin_nanf(""), __builtin_nanf(""), __builtin_nanf(""), __builtin_nanf("")};
            const v2u ob = {pack_bf16(o[0], o[1]), pack_bf16(o[2], o[3])};
            if (pass == 0) __builtin_amdgcn_raw_buffer_store_b64(ob, rws, dup(OFF_X2 + (td * 4 + fb) * 32 + fcg * 8, l >> 5), 0, AUX_SC1);
            else if (l < 32) *(v2u*)(a.out + fb * D + td * 16 + fcg * 4) = ob;
          }
        }
        __syncthreads();
      }
      if (pass == 0) { if (has_dn) SETTLE(); q_second(L, wqf, csq, rws, tq, w, l, dead, nc); hold(now(), T_Q_X3); }
    }
  }
}

__global__ __launch_bounds__(512, 2) void mega_kernel(const u16* __restrict__ x, const u16* __restrict__ K, const u16* __restrict__ V,
                                                      const u16* __restrict__ wq, const u16* __restrict__ wo, const u16* __restrict__ wg,
                                                      const u16* __restrict__ wu, const u16* __restrict__ wd, u16* __restrict__ out,
                                                      unsigned char* __restrict__ ws) {
  asm volatile("" ::"s"(x), "s"(K), "s"(V), "s"(wq), "s"(wo), "s"(wg), "s"(wu), "s"(wd), "s"(out), "s"(ws));
  __shared__ __attribute__((aligned(16))) unsigned char lds[L_END];
  LAS unsigned char* L = (LAS unsigned char*)lds;
  const int cu = blockIdx.x, xc = cu & 7, y = cu >> 3;
  const int l = threadIdx.x & 63, w = __builtin_amdgcn_readfirstlane(threadIdx.x >> 6);
  const rsrc_t rws = mkrsrc(ws, WS_USED);
  if (y < 16) attn_role(L, K, V, rws, cu, xc, y, w, l);
  else {
    WArgs a{x, wq, wo, wg, wu, wd, out};
    weight_role(L, a, rws, cu, xc, y - 16, w, l);
  }
}
}

extern "C" void kernel_launch(void* const* d_in, const int* in_sizes, int n_in, void* d_out, int out_size, void* d_ws, size_t ws_size,
                              hipStream_t stream) {
  (void)in_sizes; (void)n_in; (void)out_size; (void)ws_size;
  mega_kernel<<<256, 512, 0, stream>>>((const u16*)d_in[0], (const u16*)d_in[1], (const u16*)d_in[2], (const u16*)d_in[3],
                                       (const u16*)d_in[4], (const u16*)d_in[6], (const u16*)d_in[7], (const u16*)d_in[8],
                                       (u16*)d_out, (unsigned char*)d_ws);
}
```
